```python
import torch

def _softmax(x, axis=-1):
    return torch.softmax(x, dim=axis)
def _gelu(x):
    return torch.nn.functional.gelu(x, approximate="tanh")
def _silu(x):
    return torch.nn.functional.silu(x)
def _sigmoid(x):
    return torch.sigmoid(x)
def _layernorm(x, axis=-1, eps=1e-5):
    m = x.mean(dim=axis, keepdim=True)
    v = x.var(dim=axis, keepdim=True, unbiased=False)
    return (x - m) / torch.sqrt(v + eps)
def _scatter_add(tgt, idx, src, axis=0):
    return tgt.clone().index_add_(axis, idx, src)
def _segment_sum(src, seg, n):
    out = torch.zeros((n,) + src.shape[1:], dtype=src.dtype)
    return out.index_add_(0, seg, src)
def _pad(x, pw):
    flat = tuple(v for pair in reversed(pw) for v in pair)
    return torch.nn.functional.pad(x, flat)

kernel_name = 'parallel_sum__spectral_fnet__dense_activation_squared_relu__prompt'

def setup_inputs():

    _w = (lambda a: (lambda t: t.to(torch.bfloat16) if t.is_floating_point() else t)(torch.as_tensor(a)))
    def _A_setup():
        B, S, D = 1, 2048, 512
        ns = torch.arange(S)[:, None] * torch.arange(S)[None, :]
        nd = torch.arange(D)[:, None] * torch.arange(D)[None, :]
        Ws_re = torch.cos(-2.0 * torch.pi * ns / S).to(float)
        Ws_im = torch.sin(-2.0 * torch.pi * ns / S).to(float)
        Wd_re = torch.cos(-2.0 * torch.pi * nd / D).to(float)
        Wd_im = torch.sin(-2.0 * torch.pi * nd / D).to(float)
        return {
            "x": _w((lambda *s: torch.randn(*s, dtype=torch.bfloat16) * 0.05)(B, S, D)),
            "dft_seq_re": _w(Ws_re),
            "dft_seq_im": _w(Ws_im),
            "dft_hid_re": _w(Wd_re),
            "dft_hid_im": _w(Wd_im),
        }
    _A_in = _A_setup()

    def _B_setup():
        B, S, D, F = 1, 2048, 512, 1792
        return {
            "x": _w((lambda *s: torch.randn(*s, dtype=torch.bfloat16) * 0.05)(B, S, D)),
            "w1": _w((lambda *s: torch.randn(*s, dtype=torch.bfloat16) * 0.2)(D, F)),
            "b1": _w((lambda *s: torch.randn(*s, dtype=torch.bfloat16) * 0.2)(F)),
            "w2": _w((lambda *s: torch.randn(*s, dtype=torch.bfloat16) * 0.2)(F, D)),
            "b2": _w((lambda *s: torch.randn(*s, dtype=torch.bfloat16) * 0.2)(D)),
        }
    _B_in = _B_setup()

    return {
        "A_x": _A_in["x"],
        "A_dft_seq_re": _A_in["dft_seq_re"],
        "A_dft_seq_im": _A_in["dft_seq_im"],
        "A_dft_hid_re": _A_in["dft_hid_re"],
        "A_dft_hid_im": _A_in["dft_hid_im"],
        "B_x": _B_in["x"],
        "B_w1": _B_in["w1"],
        "B_b1": _B_in["b1"],
        "B_w2": _B_in["w2"],
        "B_b2": _B_in["b2"],
    }

def reference(A_x, A_dft_seq_re, A_dft_seq_im, A_dft_hid_re, A_dft_hid_im, B_x, B_w1, B_b1, B_w2, B_b2):
    def _A_ref(x, dft_seq_re, dft_seq_im, dft_hid_re, dft_hid_im):
        a = torch.einsum("bsd,ed->bse", x, dft_hid_re)
        b = torch.einsum("bsd,ed->bse", x, dft_hid_im)
        y = torch.einsum("ts,bse->bte", dft_seq_re, a) - torch.einsum("ts,bse->bte", dft_seq_im, b)
        return y

    def _B_ref(x, w1, b1, w2, b2):
        h = torch.einsum("bsd,df->bsf", x, w1) + b1
        zero = torch.zeros_like(h)
        r = torch.where(h > zero, h, zero)
        h = r * r
        out = torch.einsum("bsf,fd->bsd", h, w2) + b2
        return out

    x = A_x
    xn = _layernorm(x, axis=-1)
    return x + _A_ref(xn, A_dft_seq_re, A_dft_seq_im, A_dft_hid_re, A_dft_hid_im) + _B_ref(xn, B_w1, B_b1, B_w2, B_b2)
```

```cpp
#include <hip/hip_runtime.h>
#include <hip/hip_bf16.h>
#include <stdint.h>

typedef __attribute__((ext_vector_type(8))) short bf16x8;
typedef __attribute__((ext_vector_type(4))) float f32x4;
typedef unsigned short u16;

__device__ __forceinline__ float bf2f(u16 v) { return __uint_as_float(((unsigned)v) << 16); }
__device__ __forceinline__ u16 f2bf(float f) {
    __hip_bfloat16 h = __float2bfloat16(f);
    return *reinterpret_cast<u16*>(&h);
}
__device__ __forceinline__ float rbf(float f) { return bf2f(f2bf(f)); }

#define S_ 2048
#define D_ 512
#define F_ 1792

__global__ void ln_kernel(const u16* __restrict__ x, u16* __restrict__ xn) {
    int row = blockIdx.x * 4 + (threadIdx.x >> 6);
    int lane = threadIdx.x & 63;
    const u16* p = x + row * D_ + lane * 8;
    float v[8];
    bf16x8 r = *reinterpret_cast<const bf16x8*>(p);
    float s = 0;
    for (int j = 0; j < 8; ++j) { v[j] = bf2f((u16)r[j]); s += v[j]; }
    for (int o = 32; o > 0; o >>= 1) s += __shfl_xor(s, o);
    float mean = s * (1.0f / D_);
    float q = 0;
    for (int j = 0; j < 8; ++j) { float d = v[j] - mean; q += d * d; }
    for (int o = 32; o > 0; o >>= 1) q += __shfl_xor(q, o);
    float var = q * (1.0f / D_);
    float mb = rbf(mean), vb = rbf(var);
    float sd = rbf(sqrtf(rbf(vb + 1e-5f)));
    bf16x8 o;
    for (int j = 0; j < 8; ++j) { float d = rbf(v[j] - mb); o[j] = (short)f2bf(d / sd); }
    *reinterpret_cast<bf16x8*>(xn + row * D_ + lane * 8) = o;
}

template <int MODE>
__global__ void gemm_naive(const u16* __restrict__ A, int lda, const u16* __restrict__ B, int ldb, int bt,
                           int K, u16* __restrict__ C, int ldc, const u16* __restrict__ bias, int ntn) {
    int wave = blockIdx.x * 4 + (threadIdx.x >> 6);
    int lane = threadIdx.x & 63;
    int tm = wave / ntn, tn = wave % ntn;
    int fr = lane & 15, fq = lane >> 4;
    f32x4 acc = {0, 0, 0, 0};
    for (int k0 = 0; k0 < K; k0 += 32) {
        bf16x8 a = *reinterpret_cast<const bf16x8*>(A + (size_t)(tm * 16 + fr) * lda + k0 + fq * 8);
        bf16x8 b;
        if (bt) b = *reinterpret_cast<const bf16x8*>(B + (size_t)(tn * 16 + fr) * ldb + k0 + fq * 8);
        else for (int j = 0; j < 8; ++j) b[j] = (short)B[(size_t)(k0 + fq * 8 + j) * ldb + tn * 16 + fr];
        acc = __builtin_amdgcn_mfma_f32_16x16x32_bf16(a, b, acc, 0, 0, 0);
    }
    for (int j = 0; j < 4; ++j) {
        int m = tm * 16 + fq * 4 + j, n = tn * 16 + fr;
        float v = rbf(acc[j]);
        if (MODE == 1) { v = rbf(v + bf2f(bias[n])); v = v > 0 ? v : 0; v = rbf(v * v); }
        if (MODE == 2) { v = rbf(v + bf2f(bias[n])); }
        C[(size_t)m * ldc + n] = f2bf(v);
    }
}

__global__ void final_kernel(const u16* __restrict__ x, const u16* __restrict__ y1, const u16* __restrict__ y2,
                             const u16* __restrict__ o, u16* __restrict__ out, int n) {
    int i = blockIdx.x * blockDim.x + threadIdx.x;
    if (i < n) {
        float y = rbf(bf2f(y1[i]) - bf2f(y2[i]));
        float t = rbf(bf2f(x[i]) + y);
        out[i] = f2bf(t + bf2f(o[i]));
    }
}

extern "C" void kernel_launch(void* const* d_in, const int* in_sizes, int n_in,
                              void* d_out, int out_size, void* d_ws, size_t ws_size,
                              hipStream_t stream) {
    const u16* x = (const u16*)d_in[0];
    const u16* Cr = (const u16*)d_in[1];
    const u16* Ci = (const u16*)d_in[2];
    const u16* Dr = (const u16*)d_in[3];
    const u16* Di = (const u16*)d_in[4];
    const u16* w1 = (const u16*)d_in[6];
    const u16* b1 = (const u16*)d_in[7];
    const u16* w2 = (const u16*)d_in[8];
    const u16* b2 = (const u16*)d_in[9];
    u16* out = (u16*)d_out;
    u16* ws = (u16*)d_ws;
    u16* xn = ws;
    u16* a = xn + S_ * D_;
    u16* b = a + S_ * D_;
    u16* h = b + S_ * D_;
    u16* y1 = h + S_ * F_;
    u16* y2 = y1 + S_ * D_;
    u16* o = y2 + S_ * D_;
    ln_kernel<<<S_ / 4, 256, 0, stream>>>(x, xn);
    gemm_naive<0><<<(S_ / 16) * (D_ / 16) / 4, 256, 0, stream>>>(xn, D_, Dr, D_, 1, D_, a, D_, nullptr, D_ / 16);
    gemm_naive<0><<<(S_ / 16) * (D_ / 16) / 4, 256, 0, stream>>>(xn, D_, Di, D_, 1, D_, b, D_, nullptr, D_ / 16);
    gemm_naive<1><<<(S_ / 16) * (F_ / 16) / 4, 256, 0, stream>>>(xn, D_, w1, F_, 0, D_, h, F_, b1, F_ / 16);
    gemm_naive<0><<<(S_ / 16) * (D_ / 16) / 4, 256, 0, stream>>>(Cr, S_, a, D_, 0, S_, y1, D_, nullptr, D_ / 16);
    gemm_naive<0><<<(S_ / 16) * (D_ / 16) / 4, 256, 0, stream>>>(Ci, S_, b, D_, 0, S_, y2, D_, nullptr, D_ / 16);
    gemm_naive<2><<<(S_ / 16) * (D_ / 16) / 4, 256, 0, stream>>>(h, F_, w2, D_, 0, F_, o, D_, b2, D_ / 16);
    final_kernel<<<S_ * D_ / 256, 256, 0, stream>>>(x, y1, y2, o, out, S_ * D_);
}
```

```cpp
#include <hip/hip_runtime.h>
#include <hip/hip_bf16.h>
#include <stdint.h>

typedef __attribute__((ext_vector_type(8))) short bf16x8;
typedef __attribute__((ext_vector_type(4))) short bf16x4;
typedef __attribute__((ext_vector_type(4))) float f32x4;
typedef __attribute__((ext_vector_type(2))) __bf16 bfx2;
typedef __attribute__((ext_vector_type(4))) unsigned u32x4;
typedef __attribute__((ext_vector_type(2))) unsigned u32x2;
typedef unsigned short u16;
#define LDSP(p) ((__attribute__((address_space(3))) void*)(p))

#define NLD 4
#define LPRIO 3
#define S_ 2048
#define D_ 512
#define F_ 1792
#define KT_ 5888

__device__ __forceinline__ float bf2f(u16 v) { return __uint_as_float(((unsigned)v) << 16); }
__device__ __forceinline__ unsigned pack2(float lo, float hi) {
    bfx2 r = {(__bf16)lo, (__bf16)hi};
    return __builtin_bit_cast(unsigned, r);
}
__device__ __forceinline__ float rbf(float f) { return (float)(__bf16)f; }
__device__ __forceinline__ float dot2(unsigned a, unsigned b, float c) {
    return __builtin_amdgcn_fdot2_f32_bf16(__builtin_bit_cast(bfx2, a), __builtin_bit_cast(bfx2, b), c, false);
}
#define WAITV(n) asm volatile("s_waitcnt vmcnt(" #n ")" ::: "memory")
#define BAR() __builtin_amdgcn_s_barrier()
#define GLDS(src, dst) __builtin_amdgcn_global_load_lds((const void*)(src), LDSP(dst), 16, 0, 0)
#define MFMA(a, b, c) __builtin_amdgcn_mfma_f32_16x16x32_bf16(a, b, c, 0, 0, 0)

#define K1_STG 40960
#define K1_STATS (3 * K1_STG)
#define K1_CS (K1_STATS + 1024)
#define K1_B1 (K1_CS + 1024)
#define K1_W2 (K1_B1 + 1024)
#define K1_LDS (K1_W2 + 8192)

template <bool ISD>
__device__ __forceinline__ void k1_loader(char* smem, const u16* __restrict__ x, const u16* __restrict__ Dr,
                                          const u16* __restrict__ Di, const u16* __restrict__ w1,
                                          int rb, int n0, int nu, int L, int lane) {
    __builtin_amdgcn_s_setprio(3);
    const unsigned r0 = lane >> 3, c = lane & 7, hb = (lane >> 4) & 1;
    unsigned so[4];
    uint64_t pLo, pHi;
    int cross = 64;
    if (L == 0) {
#pragma unroll
        for (int q = 0; q < 4; ++q) so[q] = r0 * (D_ * 2) + (c ^ ((q << 1) | hb)) * 16;
        pLo = pHi = (uint64_t)(x + (size_t)rb * 128 * D_);
    } else {
#pragma unroll
        for (int q = 0; q < 4; ++q) {
            if (ISD) so[q] = r0 * (D_ * 2) + (c ^ (((q & 1) * 4 + (lane >> 4)) & 7)) * 16;
            else so[q] = r0 * (F_ * 2) + (c ^ ((hb | ((q & 1) << 1)) << 1)) * 16;
        }
        if (ISD) {
            const int rr = n0 + (L - 1) * 96;
            pLo = (uint64_t)Dr + (uint64_t)rr * (D_ * 2);
            pHi = (uint64_t)Di + (uint64_t)rr * (D_ * 2) - 512 * (D_ * 2);
            cross = (512 - rr) >> 3;
        } else {
            pLo = pHi = (uint64_t)(w1 + n0);
        }
    }
    const int ni = L == 0 ? 16 : (L == 1 ? 12 : 2 * nu - 12);
    char* dbase = smem + (L == 0 ? 0 : (L == 1 ? 16384 : 16384 + 12288));
#define K1_LOAD(j)                                                                                      \
    do {                                                                                                \
        char* dst = dbase + ((j) % 3) * K1_STG;                                                         \
        if (L == 0 || ISD) {                                                                            \
            _Pragma("unroll") for (int i = 0; i < 16; ++i) if (i < ni)                                  \
                GLDS((const char*)((i < cross ? pLo : pHi) + (j) * 128 + i * 8 * (D_ * 2)) + so[i & 3], dst + i * 1024); \
        } else if (L == 1) {                                                                            \
            _Pragma("unroll") for (int i = 0; i < 12; ++i)                                              \
                GLDS((const char*)(pLo + (uint64_t)(j) * 64 * (F_ * 2) + (i & 7) * 8 * (F_ * 2) + (i >> 3) * 128) + so[i & 1], dst + i * 1024); \
        } else {                                                                                        \
            _Pragma("unroll") for (int i = 12; i < 24; ++i) if (i - 12 < ni)                            \
                GLDS((const char*)(pLo + (uint64_t)(j) * 64 * (F_ * 2) + (i & 7) * 8 * (F_ * 2) + (i >> 3) * 128) + so[i & 1], dst + (i - 12) * 1024); \
        }                                                                                               \
    } while (0)
    K1_LOAD(0);
    K1_LOAD(1);
#pragma unroll 1
    for (int j = 0; j < 8; ++j) {
        if (j < 7) { if (ni == 16) WAITV(16); else if (ni == 12) WAITV(12); else WAITV(4); }
        else WAITV(0);
        BAR();
        if (j + 2 < 8) K1_LOAD(j + 2);
    }
#undef K1_LOAD
}

template <bool ISD>
__device__ __forceinline__ void k1_body(char* smem, int nu, int w, int lane) {
    const int fr = lane & 15, fq = lane >> 4;
    const int wm = w >> 1, wn = w & 1;
    const unsigned aoff = (wm * 64 + ((fr >> 2) << 3) + (fr & 3)) * 128 + ((fq ^ ((fr >> 1) & 7)) * 16);
    unsigned boff[2];
#pragma unroll
    for (int e = 0; e < 2; ++e) {
        const int u = wn * 2 + e;
        if (ISD) boff[e] = 16384 + (u * 16 + fr) * 128 + ((fq ^ ((fr >> 1) & 7)) * 16);
        else {
            const int q = fr >> 2, p = fr & 3;
            const int f = ((q >> 1) & 1) | ((fq & 1) << 1);
            boff[e] = 16384 + (fq * 8 + q) * 128 + ((u ^ f) * 32) + p * 8;
        }
    }
    f32x4 acc[4][6] = {};
    float cs[3] = {0.f, 0.f, 0.f};
    float s1[2] = {0.f, 0.f}, s2[2] = {0.f, 0.f};
    bf16x8 a0[4], b0[6], a1[4], b1f[6];
#define K1_LDF(af, bfr, j, kk)                                                                                   \
    do {                                                                                                         \
        const char* sb = smem + ((j) % 3) * K1_STG;                                                              \
        _Pragma("unroll") for (int m = 0; m < 4; ++m)                                                            \
            af[m] = *reinterpret_cast<const bf16x8*>(sb + (aoff ^ ((kk) * 64)) + (m >> 1) * 4096 + (m & 1) * 512); \
        _Pragma("unroll") for (int i = 0; i < 6; ++i) {                                                          \
            if (ISD) bfr[i] = *reinterpret_cast<const bf16x8*>(sb + (boff[i & 1] ^ ((kk) * 64)) + (i >> 1) * 8192); \
            else {                                                                                               \
                bf16x4 h0 = __builtin_amdgcn_ds_read_tr16_b64_v4i16((__attribute__((address_space(3))) bf16x4*)(sb + boff[i & 1] + (i >> 1) * 8192 + (kk) * 4096));        \
                bf16x4 h1 = __builtin_amdgcn_ds_read_tr16_b64_v4i16((__attribute__((address_space(3))) bf16x4*)(sb + boff[i & 1] + (i >> 1) * 8192 + (kk) * 4096 + 512));  \
                bfr[i] = __builtin_shufflevector(h0, h1, 0, 1, 2, 3, 4, 5, 6, 7);                                \
            }                                                                                                    \
        }                                                                                                        \
    } while (0)
#define K1_MMA(af, bfr)                                                                                          \
    do {                                                                                                         \
        _Pragma("unroll") for (int i = 0; i < 6; ++i)                                                            \
            _Pragma("unroll") for (int m = 0; m < 4; ++m)                                                        \
                acc[m][i] = ISD ? MFMA(af[m], bfr[i], acc[m][i]) : MFMA(bfr[i], af[m], acc[m][i]);               \
        _Pragma("unroll") for (int t = 0; t < 2; ++t) {                                                          \
            const u32x4 v0 = __builtin_bit_cast(u32x4, af[t]), v1 = __builtin_bit_cast(u32x4, af[2 + t]);        \
            _Pragma("unroll") for (int e = 0; e < 4; ++e) {                                                      \
                const unsigned v = wn ? v1[e] : v0[e];                                                           \
                s1[t] = dot2(v, 0x3f803f80u, s1[t]); s2[t] = dot2(v, v, s2[t]);                                  \
            }                                                                                                    \
        }                                                                                                        \
        _Pragma("unroll") for (int i = 0; i < 3; ++i) {                                                          \
            const u32x4 v0 = __builtin_bit_cast(u32x4, bfr[i]), v1 = __builtin_bit_cast(u32x4, bfr[3 + i]);      \
            _Pragma("unroll") for (int e = 0; e < 4; ++e) cs[i] = dot2(wm ? v1[e] : v0[e], 0x3f803f80u, cs[i]);  \
        }                                                                                                        \
        _Pragma("unroll") for (int q = 0; q < 24; ++q) {                                                         \
            __builtin_amdgcn_sched_group_barrier(0x008, 1, 0);                                                   \
            if (q < (ISD ? 10 : 16)) __builtin_amdgcn_sched_group_barrier(0x100, 1, 0);                          \
            __builtin_amdgcn_sched_group_barrier(0x002, 2, 0);                                                   \
        }                                                                                                        \
    } while (0)
    BAR();
    K1_LDF(a0, b0, 0, 0);
#pragma unroll 1
    for (int j = 0; j < 7; ++j) {
        K1_LDF(a1, b1f, j, 1);
        K1_MMA(a0, b0);
        BAR();
        K1_LDF(a0, b0, j + 1, 0);
        K1_MMA(a1, b1f);
    }
    K1_LDF(a1, b1f, 7, 1);
    K1_MMA(a0, b0);
    K1_MMA(a1, b1f);
    BAR();
    float* stats = (float*)(smem + K1_STATS);
    float* csp = (float*)(smem + K1_CS);
#pragma unroll
    for (int t = 0; t < 2; ++t) {
        float a = s1[t], b = s2[t];
        a += __shfl_xor(a, 16); a += __shfl_xor(a, 32);
        b += __shfl_xor(b, 16); b += __shfl_xor(b, 32);
        const float mean = a * (1.0f / D_);
        const float var = fmaxf(b * (1.0f / D_) - mean * mean, 0.f);
        const float sd = rbf(sqrtf(rbf(rbf(var) + 1e-5f)));
        const int r = wm * 64 + wn * 32 + ((fr >> 2) << 3) + 4 * t + (fr & 3);
        if (fq == 0) { stats[r * 2] = rbf(mean); stats[r * 2 + 1] = 1.0f / sd; }
    }
#pragma unroll
    for (int i = 0; i < 3; ++i) {
        float c = cs[i];
        c += __shfl_xor(c, 16); c += __shfl_xor(c, 32);
        const int ii = wm * 3 + i;
        if (fq == 0) csp[((ii >> 1) * 4 + wn * 2 + (ii & 1)) * 16 + fr] = c;
    }
    {
        char* wb = ISD ? smem + (wn * 32 + fr) * 528 + (wm * 64 + fq * 8) * 4
                       : smem + (wm * 64 + ((fr >> 2) << 3) + (fr & 3)) * 784 + (wn * 32 + fq * 4) * 4;
#pragma unroll
        for (int i = 0; i < 6; ++i)
#pragma unroll
            for (int m = 0; m < 4; ++m) {
                const int uo = (i >> 1) * 4 + (i & 1);
                if (ISD) *reinterpret_cast<f32x4*>(wb + uo * 16 * 528 + (m >> 1) * 128 + (m & 1) * 16) = acc[m][i];
                else *reinterpret_cast<f32x4*>(wb + ((m >> 1) * 32 + (m & 1) * 4) * 784 + uo * 64) = acc[m][i];
            }
    }
    asm volatile("s_waitcnt lgkmcnt(0)" ::: "memory");
}

template <bool ISD>
__device__ __forceinline__ void k1_out(char* smem, u16* __restrict__ BT, u16* __restrict__ H, int rb, int n0, int nu, int w, int lane) {
    const float* stats = (const float*)(smem + K1_STATS);
    const float* csp = (const float*)(smem + K1_CS);
    const int nh = nu >> 1;
    if (ISD) {
        const int c = lane & 15;
        f32x4 st[4];
#pragma unroll
        for (int e = 0; e < 4; ++e) st[e] = *reinterpret_cast<const f32x4*>(stats + c * 16 + e * 4);
#pragma unroll 2
        for (int q = 0; q < nh; ++q) {
            const int e0 = (q * 8 + w) * 4 + (lane >> 4);
            const f32x4 p0 = *reinterpret_cast<const f32x4*>(smem + e0 * 528 + c * 32);
            const f32x4 p1 = *reinterpret_cast<const f32x4*>(smem + e0 * 528 + c * 32 + 16);
            const float cc = csp[e0];
            const int nn = n0 + e0;
            const float sg = nn < 512 ? 1.f : -1.f;
            float v[8];
#pragma unroll
            for (int e = 0; e < 8; ++e) {
                const float mu = st[e >> 1][(e & 1) * 2], ri = st[e >> 1][(e & 1) * 2 + 1];
                v[e] = ((e < 4 ? p0[e & 3] : p1[e & 3]) - mu * cc) * (sg * ri);
            }
            u32x4 o;
#pragma unroll
            for (int e = 0; e < 4; ++e) o[e] = pack2(v[2 * e], v[2 * e + 1]);
            *reinterpret_cast<u32x4*>(BT + (size_t)(nn & 511) * KT_ + (nn < 512 ? 0 : 2048) + rb * 128 + c * 8) = o;
        }
    } else {
#pragma unroll 2
        for (int q = 0; q < nh; ++q) {
            const int id = q * 8 + (lane >> 3);
            const int ln = nu == 12 ? id % 3 : id & 1;
            const int r = w * 16 + (nu == 12 ? id / 3 : id >> 1);
            const int f0 = ln * 64 + (lane & 7) * 8;
            const f32x4 p0 = *reinterpret_cast<const f32x4*>(smem + r * 784 + f0 * 4);
            const f32x4 p1 = *reinterpret_cast<const f32x4*>(smem + r * 784 + f0 * 4 + 16);
            const f32x4 c0 = *reinterpret_cast<const f32x4*>(csp + f0);
            const f32x4 c1 = *reinterpret_cast<const f32x4*>(csp + f0 + 4);
            const u32x4 bv = *reinterpret_cast<const u32x4*>(smem + K1_B1 + f0 * 2);
            const float mu = stats[r * 2], ri = stats[r * 2 + 1];
            float v[8];
#pragma unroll
            for (int e = 0; e < 8; ++e) {
                const float bb = bf2f((u16)(bv[e >> 1] >> ((e & 1) * 16)));
                float z = ((e < 4 ? p0[e & 3] : p1[e & 3]) - mu * (e < 4 ? c0[e & 3] : c1[e & 3])) * ri + bb;
                z = fmaxf(z, 0.f);
                v[e] = z * z;
            }
            u32x4 o;
#pragma unroll
            for (int e = 0; e < 4; ++e) o[e] = pack2(v[2 * e], v[2 * e + 1]);
            *reinterpret_cast<u32x4*>(H + (size_t)(rb * 128 + r) * F_ + n0 + f0) = o;
        }
    }
}

__global__ __launch_bounds__(512, 2) void k1(const u16* __restrict__ x, const u16* __restrict__ Dr,
                                             const u16* __restrict__ Di, const u16* __restrict__ w1,
                                             const u16* __restrict__ b1, const u16* __restrict__ w2,
                                             u16* __restrict__ BT, u16* __restrict__ H, unsigned* __restrict__ cnt) {
    extern __shared__ __attribute__((aligned(16))) char smem[];
    const int tid = threadIdx.x, lane = tid & 63, w = __builtin_amdgcn_readfirstlane(tid >> 6);
    const int bid = blockIdx.x, xcd = bid & 7, idx = bid >> 3;
    const int rb = (xcd & 1) * 8 + (idx >> 2);
    const int ct = (xcd >> 1) * 4 + (idx & 3);
    if (bid == 255 && w == 7) cnt[lane * 16] = 0u;
    const int fb = bid >> 3, eb = bid & 7;
    const bool isd = ct < 6;
    const int c2 = ct - 6;
    const int n0 = 16 * (isd ? (ct < 4 ? 12 * ct : 48 + 8 * (ct - 4)) : (c2 < 8 ? 12 * c2 : 96 + 8 * (c2 - 8)));
    const int nu = isd ? (ct < 4 ? 12 : 8) : (c2 < 8 ? 12 : 8);
    if (w >= 4) {
        if (w == 7) {
            if (bid < 224) {
#pragma unroll
                for (int i = 0; i < 8; ++i)
                    GLDS(w2 + (size_t)(fb * 64 + i * 8 + (lane >> 3)) * D_ + eb * 64 + (lane & 7) * 8, smem + K1_W2 + i * 1024);
            }
            if (!isd && lane < nu * 2) GLDS(b1 + n0 + lane * 8, smem + K1_B1);
            WAITV(0);
#pragma unroll 1
            for (int j = 0; j < 8; ++j) BAR();
        } else {
            if (isd) k1_loader<true>(smem, x, Dr, Di, w1, rb, n0, nu, w - 4, lane);
            else k1_loader<false>(smem, x, Dr, Di, w1, rb, n0, nu, w - 4, lane);
            __builtin_amdgcn_s_setprio(0);
        }
        BAR();
    } else {
        if (isd) k1_body<true>(smem, nu, w, lane);
        else k1_body<false>(smem, nu, w, lane);
    }
    BAR();
    if (isd) k1_out<true>(smem, BT, H, rb, n0, nu, w, lane);
    else k1_out<false>(smem, BT, H, rb, n0, nu, w, lane);
    if (bid < 224) {
        const int fr = lane & 15, fq = lane >> 4, q = fr >> 2, p = fr & 3;
        const int eu = w & 3, fh = w >> 2;
        bf16x4 h[2];
#pragma unroll
        for (int t2 = 0; t2 < 2; ++t2) {
            const int f = fh * 32 + fq * 8 + t2 * 4 + q;
            h[t2] = __builtin_amdgcn_ds_read_tr16_b64_v4i16(
                (__attribute__((address_space(3))) bf16x4*)(smem + K1_W2 + f * 128 + eu * 32 + p * 8));
        }
        bf16x8 o = __builtin_shufflevector(h[0], h[1], 0, 1, 2, 3, 4, 5, 6, 7);
        *reinterpret_cast<bf16x8*>(BT + (size_t)(eb * 64 + eu * 16 + fr) * KT_ + 4096 + fb * 64 + fh * 32 + fq * 8) = o;
    }
}

__global__ __launch_bounds__(256 + 64 * NLD, 2) void k2(const u16* __restrict__ Cr, const u16* __restrict__ Ci,
                                             const u16* __restrict__ H, const u16* __restrict__ BT,
                                             float* __restrict__ XB, unsigned* __restrict__ cnt,
                                             const u16* __restrict__ x, const u16* __restrict__ b2,
                                             u16* __restrict__ out) {
    extern __shared__ __attribute__((aligned(16))) char smem[];
    const int tid = threadIdx.x, lane = tid & 63, w = __builtin_amdgcn_readfirstlane(tid >> 6);
    const int bid = blockIdx.x, xcd = bid & 7, idx = bid >> 3;
    const int ksl = xcd >> 1;
    const int rb = (xcd & 1) * 8 + (idx >> 2);
    const int nt = idx & 3;
    constexpr int NK = 23;
    if (w >= 4) {
        const bool isA = ((w - 4) < NLD / 2);
        const int part = (w - 4) % (NLD / 2);
        __builtin_amdgcn_s_setprio(LPRIO);
        const uint64_t pA0 = (uint64_t)(Cr + (size_t)(rb * 128) * S_);
        const uint64_t pA1 = (uint64_t)(Ci + (size_t)(rb * 128) * S_) - 32 * 128;
        const uint64_t pA2 = (uint64_t)(H + (size_t)(rb * 128) * F_) - 64 * 128;
        const uint64_t pB = (uint64_t)(BT + (size_t)(nt * 128) * KT_);
        const unsigned r0 = lane >> 3;
        const unsigned c0 = ((lane & 7) ^ ((lane >> 4) & 7)) * 16, c1 = ((lane & 7) ^ ((4 + (lane >> 4)) & 7)) * 16;
#define K2_LOAD(j)                                                                   \
    do {                                                                             \
        const int kb = ksl * NK + (j);                                               \
        char* dst = smem + ((j) & 3) * 32768 + (isA ? 0 : 16384) + part * (32 / NLD) * 1024; \
        uint64_t base = kb < 32 ? pA0 : (kb < 64 ? pA1 : pA2);                       \
        unsigned ldb = kb < 64 ? S_ * 2 : F_ * 2;                                    \
        if (!isA) { base = pB; ldb = KT_ * 2; }                                      \
        base += kb * 128 + (uint64_t)(part * (32 / NLD) * 8) * ldb;                  \
        const unsigned o0 = r0 * ldb + c0, o1 = r0 * ldb + c1;                       \
        _Pragma("unroll") for (int i = 0; i < 32 / NLD; ++i)                         \
            GLDS((const char*)(base + (uint64_t)(i * 8) * ldb) + ((i & 1) ? o1 : o0), dst + i * 1024); \
    } while (0)
        K2_LOAD(0); K2_LOAD(1); K2_LOAD(2);
        if (NLD == 2) WAITV(32); else WAITV(16);
        BAR();
#pragma unroll 1
        for (int j = 0; j < NK - 1; ++j) {
            if (j + 2 < NK) { if (NLD == 2) WAITV(16); else WAITV(8); } else WAITV(0);
            BAR();
            if (j + 3 < NK) K2_LOAD(j + 3);
        }
        return;
    }
    const int fr = lane & 15, fq = lane >> 4;
    const int wm = w >> 1, wn = w & 1;
    f32x4 acc[4][4] = {};
    const unsigned aoff = (wm * 64 + fr) * 128 + ((fq ^ ((fr >> 1) & 7)) * 16);
    const unsigned boff = 16384 + (wn * 64 + fr) * 128 + ((fq ^ ((fr >> 1) & 7)) * 16);
    bf16x8 a0[4], b0[4], a1[4], b1[4];
#define K2_LDF(af, bf, j, kk)                                                                                  \
    do {                                                                                                       \
        const char* sa = smem + ((j) & 3) * 32768;                                                             \
        _Pragma("unroll") for (int m = 0; m < 4; ++m) {                                                        \
            af[m] = *reinterpret_cast<const bf16x8*>(sa + (aoff ^ ((kk) * 64)) + m * 2048);                    \
            bf[m] = *reinterpret_cast<const bf16x8*>(sa + (boff ^ ((kk) * 64)) + m * 2048);                    \
        }                                                                                                      \
    } while (0)
#define K2_MMA(af, bf)                                                               \
    do {                                                                             \
        _Pragma("unroll") for (int m = 0; m < 4; ++m)                                \
            _Pragma("unroll") for (int n = 0; n < 4; ++n) acc[m][n] = MFMA(bf[n], af[m], acc[m][n]); \
    } while (0)
#define K2_SCHED()                                                                   \
    do {                                                                             \
        _Pragma("unroll") for (int q = 0; q < 8; ++q) {                              \
            __builtin_amdgcn_sched_group_barrier(0x100, 1, 0);                       \
            __builtin_amdgcn_sched_group_barrier(0x008, 2, 0);                       \
        }                                                                            \
    } while (0)
    BAR();
    K2_LDF(a0, b0, 0, 0);
#pragma unroll 1
    for (int j = 0; j < NK - 1; ++j) {
        K2_LDF(a1, b1, j, 1);
        K2_MMA(a0, b0);
        K2_SCHED();
        BAR();
        K2_LDF(a0, b0, j + 1, 0);
        K2_MMA(a1, b1);
        K2_SCHED();
    }
    K2_LDF(a1, b1, NK - 1, 1);
    K2_MMA(a0, b0);
    K2_MMA(a1, b1);
    const int tile = rb * 4 + nt;
    const __amdgpu_buffer_rsrc_t xr = __builtin_amdgcn_make_buffer_rsrc((void*)XB, 0, 64 * 262144, 0x00020000);
    const unsigned xo = (unsigned)tile * 262144u + (unsigned)w * 4096u + (unsigned)lane * 16u;
#pragma unroll
    for (int q = 0; q < 4; ++q) {
        if (q != ksl) {
#pragma unroll
            for (int n = 0; n < 4; ++n)
                __builtin_amdgcn_raw_buffer_store_b128(__builtin_bit_cast(u32x4, acc[q][n]), xr, xo + (q * 4 + ksl) * 16384 + n * 1024, 0, 16);
        }
    }
    f32x4 own[4];
#pragma unroll
    for (int n = 0; n < 4; ++n) own[n] = ksl == 0 ? acc[0][n] : (ksl == 1 ? acc[1][n] : (ksl == 2 ? acc[2][n] : acc[3][n]));
    const int row = rb * 128 + wm * 64 + ksl * 16 + fr;
    const int col = nt * 128 + wn * 64 + fq * 4;
    u32x2 xv[4], bv[4];
#pragma unroll
    for (int n = 0; n < 4; ++n) {
        xv[n] = *reinterpret_cast<const u32x2*>(x + (size_t)row * D_ + col + n * 16);
        bv[n] = *reinterpret_cast<const u32x2*>(b2 + col + n * 16);
    }
    WAITV(0);
    BAR();
    if (tid == 0) {
        __hip_atomic_fetch_add(cnt + tile * 16, 1u, __ATOMIC_RELAXED, __HIP_MEMORY_SCOPE_AGENT);
        for (unsigned spin = 0; spin < (1u << 24); ++spin) {
            if (__hip_atomic_load(cnt + tile * 16, __ATOMIC_RELAXED, __HIP_MEMORY_SCOPE_AGENT) >= 4u) break;
            __builtin_amdgcn_s_sleep(1);
        }
    }
    BAR();
    {
        u32x4 pv[3][4];
#pragma unroll
        for (int i = 0; i < 3; ++i) {
            const int p = (ksl + 1 + i) & 3;
#pragma unroll
            for (int n = 0; n < 4; ++n) pv[i][n] = __builtin_amdgcn_raw_buffer_load_b128(xr, xo + (ksl * 4 + p) * 16384 + n * 1024, 0, 16);
        }
#pragma unroll
        for (int i = 0; i < 3; ++i)
#pragma unroll
            for (int n = 0; n < 4; ++n) own[n] += __builtin_bit_cast(f32x4, pv[i][n]);
    }
#pragma unroll
    for (int n = 0; n < 4; ++n) {
        float r[4];
#pragma unroll
        for (int e = 0; e < 4; ++e)
            r[e] = own[n][e] + bf2f((u16)(xv[n][e >> 1] >> ((e & 1) * 16))) + bf2f((u16)(bv[n][e >> 1] >> ((e & 1) * 16)));
        u32x2 o;
        o[0] = pack2(r[0], r[1]);
        o[1] = pack2(r[2], r[3]);
        *reinterpret_cast<u32x2*>(out + (size_t)row * D_ + col + n * 16) = o;
    }
}

extern "C" void kernel_launch(void* const* d_in, const int* in_sizes, int n_in,
                              void* d_out, int out_size, void* d_ws, size_t ws_size,
                              hipStream_t stream) {
    const u16* x = (const u16*)d_in[0];
    const u16* Cr = (const u16*)d_in[1];
    const u16* Ci = (const u16*)d_in[2];
    const u16* Dr = (const u16*)d_in[3];
    const u16* Di = (const u16*)d_in[4];
    const u16* w1 = (const u16*)d_in[6];
    const u16* b1 = (const u16*)d_in[7];
    const u16* w2 = (const u16*)d_in[8];
    const u16* b2 = (const u16*)d_in[9];
    u16* out = (u16*)d_out;
    char* ws = (char*)d_ws;
    u16* BT = (u16*)ws;
    u16* H = (u16*)(ws + 6291456);
    float* XB = (float*)(ws + 6291456 + 7340032);
    unsigned* cnt = (unsigned*)(ws + 6291456 + 7340032 + 16777216);
    (void)hipFuncSetAttribute((const void*)k1, hipFuncAttributeMaxDynamicSharedMemorySize, K1_LDS);
    (void)hipFuncSetAttribute((const void*)k2, hipFuncAttributeMaxDynamicSharedMemorySize, 131072);
    k1<<<256, 512, K1_LDS, stream>>>(x, Dr, Di, w1, b1, w2, BT, H, cnt);
    k2<<<256, 256 + 64 * NLD, 131072, stream>>>(Cr, Ci, H, BT, XB, cnt, x, b2, out);
}
```

```cpp
#include <hip/hip_runtime.h>
#include <hip/hip_bf16.h>
#include <stdint.h>

typedef __attribute__((ext_vector_type(8))) short bf16x8;
typedef __attribute__((ext_vector_type(4))) short bf16x4;
typedef __attribute__((ext_vector_type(4))) float f32x4;
typedef __attribute__((ext_vector_type(2))) __bf16 bfx2;
typedef __attribute__((ext_vector_type(4))) unsigned u32x4;
typedef __attribute__((ext_vector_type(2))) unsigned u32x2;
typedef unsigned short u16;
#define LDSP(p) ((__attribute__((address_space(3))) void*)(p))

#define NLD 4
#define LPRIO 3
#define PFG 2
#define S_ 2048
#define D_ 512
#define F_ 1792
#define KT_ 5888

__device__ __forceinline__ float bf2f(u16 v) { return __uint_as_float(((unsigned)v) << 16); }
__device__ __forceinline__ unsigned pack2(float lo, float hi) {
    bfx2 r = {(__bf16)lo, (__bf16)hi};
    return __builtin_bit_cast(unsigned, r);
}
__device__ __forceinline__ float rbf(float f) { return (float)(__bf16)f; }
__device__ __forceinline__ float dot2(unsigned a, unsigned b, float c) {
    return __builtin_amdgcn_fdot2_f32_bf16(__builtin_bit_cast(bfx2, a), __builtin_bit_cast(bfx2, b), c, false);
}
#define WAITV(n) asm volatile("s_waitcnt vmcnt(" #n ")" ::: "memory")
#define BAR() __builtin_amdgcn_s_barrier()
#define GLDS(src, dst) __builtin_amdgcn_global_load_lds((const void*)(src), LDSP(dst), 16, 0, 0)
#define MFMA(a, b, c) __builtin_amdgcn_mfma_f32_16x16x32_bf16(a, b, c, 0, 0, 0)

#define K1_STG 40960
#define K1_STATS (3 * K1_STG)
#define K1_CS (K1_STATS + 1024)
#define K1_B1 (K1_CS + 1024)
#define K1_W2 (K1_B1 + 1024)
#define K1_LDS (K1_W2 + 8192)

template <bool ISD>
__device__ __forceinline__ void k1_loader(char* smem, const u16* __restrict__ x, const u16* __restrict__ Dr,
                                          const u16* __restrict__ Di, const u16* __restrict__ w1,
                                          int rb, int n0, int nu, int L, int lane) {
    __builtin_amdgcn_s_setprio(3);
    const unsigned r0 = lane >> 3, c = lane & 7, hb = (lane >> 4) & 1;
    unsigned so[4];
    uint64_t pLo, pHi;
    int cross = 64;
    if (L == 0) {
#pragma unroll
        for (int q = 0; q < 4; ++q) so[q] = r0 * (D_ * 2) + (c ^ ((q << 1) | hb)) * 16;
        pLo = pHi = (uint64_t)(x + (size_t)rb * 128 * D_);
    } else {
#pragma unroll
        for (int q = 0; q < 4; ++q) {
            if (ISD) so[q] = r0 * (D_ * 2) + (c ^ (((q & 1) * 4 + (lane >> 4)) & 7)) * 16;
            else so[q] = r0 * (F_ * 2) + (c ^ ((hb | ((q & 1) << 1)) << 1)) * 16;
        }
        if (ISD) {
            const int rr = n0 + (L - 1) * 96;
            pLo = (uint64_t)Dr + (uint64_t)rr * (D_ * 2);
            pHi = (uint64_t)Di + (uint64_t)rr * (D_ * 2) - 512 * (D_ * 2);
            cross = (512 - rr) >> 3;
        } else {
            pLo = pHi = (uint64_t)(w1 + n0);
        }
    }
    const int ni = L == 0 ? 16 : (L == 1 ? 12 : 2 * nu - 12);
    char* dbase = smem + (L == 0 ? 0 : (L == 1 ? 16384 : 16384 + 12288));
#define K1_LOAD(j)                                                                                      \
    do {                                                                                                \
        char* dst = dbase + ((j) % 3) * K1_STG;                                                         \
        if (L == 0 || ISD) {                                                                            \
            _Pragma("unroll") for (int i = 0; i < 16; ++i) if (i < ni)                                  \
                GLDS((const char*)((i < cross ? pLo : pHi) + (j) * 128 + i * 8 * (D_ * 2)) + so[i & 3], dst + i * 1024); \
        } else if (L == 1) {                                                                            \
            _Pragma("unroll") for (int i = 0; i < 12; ++i)                                              \
                GLDS((const char*)(pLo + (uint64_t)(j) * 64 * (F_ * 2) + (i & 7) * 8 * (F_ * 2) + (i >> 3) * 128) + so[i & 1], dst + i * 1024); \
        } else {                                                                                        \
            _Pragma("unroll") for (int i = 12; i < 24; ++i) if (i - 12 < ni)                            \
                GLDS((const char*)(pLo + (uint64_t)(j) * 64 * (F_ * 2) + (i & 7) * 8 * (F_ * 2) + (i >> 3) * 128) + so[i & 1], dst + (i - 12) * 1024); \
        }                                                                                               \
    } while (0)
    K1_LOAD(0);
    K1_LOAD(1);
#pragma unroll 1
    for (int j = 0; j < 8; ++j) {
        if (j < 7) { if (ni == 16) WAITV(16); else if (ni == 12) WAITV(12); else WAITV(4); }
        else WAITV(0);
        BAR();
        if (j + 2 < 8) K1_LOAD(j + 2);
    }
#undef K1_LOAD
}

template <bool ISD>
__device__ __forceinline__ void k1_body(char* smem, int nu, int w, int lane) {
    const int fr = lane & 15, fq = lane >> 4;
    const int wm = w >> 1, wn = w & 1;
    const unsigned aoff = (wm * 64 + ((fr >> 2) << 3) + (fr & 3)) * 128 + ((fq ^ ((fr >> 1) & 7)) * 16);
    unsigned boff[2];
#pragma unroll
    for (int e = 0; e < 2; ++e) {
        const int u = wn * 2 + e;
        if (ISD) boff[e] = 16384 + (u * 16 + fr) * 128 + ((fq ^ ((fr >> 1) & 7)) * 16);
        else {
            const int q = fr >> 2, p = fr & 3;
            const int f = ((q >> 1) & 1) | ((fq & 1) << 1);
            boff[e] = 16384 + (fq * 8 + q) * 128 + ((u ^ f) * 32) + p * 8;
        }
    }
    f32x4 acc[4][6] = {};
    float cs[3] = {0.f, 0.f, 0.f};
    float s1[2] = {0.f, 0.f}, s2[2] = {0.f, 0.f};
    bf16x8 a0[4], b0[6], a1[4], b1f[6];
#define K1_LDF(af, bfr, j, kk)                                                                                   \
    do {                                                                                                         \
        const char* sb = smem + ((j) % 3) * K1_STG;                                                              \
        _Pragma("unroll") for (int m = 0; m < 4; ++m)                                                            \
            af[m] = *reinterpret_cast<const bf16x8*>(sb + (aoff ^ ((kk) * 64)) + (m >> 1) * 4096 + (m & 1) * 512); \
        _Pragma("unroll") for (int i = 0; i < 6; ++i) {                                                          \
            if (ISD) bfr[i] = *reinterpret_cast<const bf16x8*>(sb + (boff[i & 1] ^ ((kk) * 64)) + (i >> 1) * 8192); \
            else {                                                                                               \
                bf16x4 h0 = __builtin_amdgcn_ds_read_tr16_b64_v4i16((__attribute__((address_space(3))) bf16x4*)(sb + boff[i & 1] + (i >> 1) * 8192 + (kk) * 4096));        \
                bf16x4 h1 = __builtin_amdgcn_ds_read_tr16_b64_v4i16((__attribute__((address_space(3))) bf16x4*)(sb + boff[i & 1] + (i >> 1) * 8192 + (kk) * 4096 + 512));  \
                bfr[i] = __builtin_shufflevector(h0, h1, 0, 1, 2, 3, 4, 5, 6, 7);                                \
            }                                                                                                    \
        }                                                                                                        \
    } while (0)
#define K1_MMA(af, bfr)                                                                                          \
    do {                                                                                                         \
        _Pragma("unroll") for (int i = 0; i < 6; ++i)                                                            \
            _Pragma("unroll") for (int m = 0; m < 4; ++m)                                                        \
                acc[m][i] = ISD ? MFMA(af[m], bfr[i], acc[m][i]) : MFMA(bfr[i], af[m], acc[m][i]);               \
        _Pragma("unroll") for (int t = 0; t < 2; ++t) {                                                          \
            const u32x4 v0 = __builtin_bit_cast(u32x4, af[t]), v1 = __builtin_bit_cast(u32x4, af[2 + t]);        \
            _Pragma("unroll") for (int e = 0; e < 4; ++e) {                                                      \
                const unsigned v = wn ? v1[e] : v0[e];                                                           \
                s1[t] = dot2(v, 0x3f803f80u, s1[t]); s2[t] = dot2(v, v, s2[t]);                                  \
            }                                                                                                    \
        }                                                                                                        \
        _Pragma("unroll") for (int i = 0; i < 3; ++i) {                                                          \
            const u32x4 v0 = __builtin_bit_cast(u32x4, bfr[i]), v1 = __builtin_bit_cast(u32x4, bfr[3 + i]);      \
            _Pragma("unroll") for (int e = 0; e < 4; ++e) cs[i] = dot2(wm ? v1[e] : v0[e], 0x3f803f80u, cs[i]);  \
        }                                                                                                        \
        _Pragma("unroll") for (int q = 0; q < 24; ++q) {                                                         \
            __builtin_amdgcn_sched_group_barrier(0x008, 1, 0);                                                   \
            if (q < (ISD ? 10 : 16)) __builtin_amdgcn_sched_group_barrier(0x100, 1, 0);                          \
            __builtin_amdgcn_sched_group_barrier(0x002, 2, 0);                                                   \
        }                                                                                                        \
    } while (0)
    BAR();
    K1_LDF(a0, b0, 0, 0);
#pragma unroll 1
    for (int j = 0; j < 7; ++j) {
        K1_LDF(a1, b1f, j, 1);
        K1_MMA(a0, b0);
        BAR();
        K1_LDF(a0, b0, j + 1, 0);
        K1_MMA(a1, b1f);
    }
    K1_LDF(a1, b1f, 7, 1);
    K1_MMA(a0, b0);
    K1_MMA(a1, b1f);
    BAR();
    float* stats = (float*)(smem + K1_STATS);
    float* csp = (float*)(smem + K1_CS);
#pragma unroll
    for (int t = 0; t < 2; ++t) {
        float a = s1[t], b = s2[t];
        a += __shfl_xor(a, 16); a += __shfl_xor(a, 32);
        b += __shfl_xor(b, 16); b += __shfl_xor(b, 32);
        const float mean = a * (1.0f / D_);
        const float var = fmaxf(b * (1.0f / D_) - mean * mean, 0.f);
        const float sd = rbf(sqrtf(rbf(rbf(var) + 1e-5f)));
        const int r = wm * 64 + wn * 32 + ((fr >> 2) << 3) + 4 * t + (fr & 3);
        if (fq == 0) { stats[r * 2] = rbf(mean); stats[r * 2 + 1] = 1.0f / sd; }
    }
#pragma unroll
    for (int i = 0; i < 3; ++i) {
        float c = cs[i];
        c += __shfl_xor(c, 16); c += __shfl_xor(c, 32);
        const int ii = wm * 3 + i;
        if (fq == 0) csp[((ii >> 1) * 4 + wn * 2 + (ii & 1)) * 16 + fr] = c;
    }
    {
        char* wb = ISD ? smem + (wn * 32 + fr) * 528 + (wm * 64 + fq * 8) * 4
                       : smem + (wm * 64 + ((fr >> 2) << 3) + (fr & 3)) * 784 + (wn * 32 + fq * 4) * 4;
#pragma unroll
        for (int i = 0; i < 6; ++i)
#pragma unroll
            for (int m = 0; m < 4; ++m) {
                const int uo = (i >> 1) * 4 + (i & 1);
                if (ISD) *reinterpret_cast<f32x4*>(wb + uo * 16 * 528 + (m >> 1) * 128 + (m & 1) * 16) = acc[m][i];
                else *reinterpret_cast<f32x4*>(wb + ((m >> 1) * 32 + (m & 1) * 4) * 784 + uo * 64) = acc[m][i];
            }
    }
    asm volatile("s_waitcnt lgkmcnt(0)" ::: "memory");
}

template <bool ISD>
__device__ __forceinline__ void k1_out(char* smem, u16* __restrict__ BT, u16* __restrict__ H, int rb, int n0, int nu, int w, int lane) {
    const float* stats = (const float*)(smem + K1_STATS);
    const float* csp = (const float*)(smem + K1_CS);
    const int nh = nu >> 1;
    if (ISD) {
        const int c = lane & 15;
        f32x4 st[4];
#pragma unroll
        for (int e = 0; e < 4; ++e) st[e] = *reinterpret_cast<const f32x4*>(stats + c * 16 + e * 4);
#pragma unroll 2
        for (int q = 0; q < nh; ++q) {
            const int e0 = (q * 8 + w) * 4 + (lane >> 4);
            const f32x4 p0 = *reinterpret_cast<const f32x4*>(smem + e0 * 528 + c * 32);
            const f32x4 p1 = *reinterpret_cast<const f32x4*>(smem + e0 * 528 + c * 32 + 16);
            const float cc = csp[e0];
            const int nn = n0 + e0;
            const float sg = nn < 512 ? 1.f : -1.f;
            float v[8];
#pragma unroll
            for (int e = 0; e < 8; ++e) {
                const float mu = st[e >> 1][(e & 1) * 2], ri = st[e >> 1][(e & 1) * 2 + 1];
                v[e] = ((e < 4 ? p0[e & 3] : p1[e & 3]) - mu * cc) * (sg * ri);
            }
            u32x4 o;
#pragma unroll
            for (int e = 0; e < 4; ++e) o[e] = pack2(v[2 * e], v[2 * e + 1]);
            *reinterpret_cast<u32x4*>(BT + (size_t)(nn & 511) * KT_ + (nn < 512 ? 0 : 2048) + rb * 128 + c * 8) = o;
        }
    } else {
#pragma unroll 2
        for (int q = 0; q < nh; ++q) {
            const int id = q * 8 + (lane >> 3);
            const int ln = nu == 12 ? id % 3 : id & 1;
            const int r = w * 16 + (nu == 12 ? id / 3 : id >> 1);
            const int f0 = ln * 64 + (lane & 7) * 8;
            const f32x4 p0 = *reinterpret_cast<const f32x4*>(smem + r * 784 + f0 * 4);
            const f32x4 p1 = *reinterpret_cast<const f32x4*>(smem + r * 784 + f0 * 4 + 16);
            const f32x4 c0 = *reinterpret_cast<const f32x4*>(csp + f0);
            const f32x4 c1 = *reinterpret_cast<const f32x4*>(csp + f0 + 4);
            const u32x4 bv = *reinterpret_cast<const u32x4*>(smem + K1_B1 + f0 * 2);
            const float mu = stats[r * 2], ri = stats[r * 2 + 1];
            float v[8];
#pragma unroll
            for (int e = 0; e < 8; ++e) {
                const float bb = bf2f((u16)(bv[e >> 1] >> ((e & 1) * 16)));
                float z = ((e < 4 ? p0[e & 3] : p1[e & 3]) - mu * (e < 4 ? c0[e & 3] : c1[e & 3])) * ri + bb;
                z = fmaxf(z, 0.f);
                v[e] = z * z;
            }
            u32x4 o;
#pragma unroll
            for (int e = 0; e < 4; ++e) o[e] = pack2(v[2 * e], v[2 * e + 1]);
            *reinterpret_cast<u32x4*>(H + (size_t)(rb * 128 + r) * F_ + n0 + f0) = o;
        }
    }
}

__global__ __launch_bounds__(512, 2) void k1(const u16* __restrict__ x, const u16* __restrict__ Dr,
                                             const u16* __restrict__ Di, const u16* __restrict__ w1,
                                             const u16* __restrict__ b1, const u16* __restrict__ w2,
                                             u16* __restrict__ BT, u16* __restrict__ H, unsigned* __restrict__ cnt,
                                             const u16* __restrict__ Cr, const u16* __restrict__ Ci) {
    extern __shared__ __attribute__((aligned(16))) char smem[];
    const int tid = threadIdx.x, lane = tid & 63, w = __builtin_amdgcn_readfirstlane(tid >> 6);
    const int bid = blockIdx.x, xcd = bid & 7, idx = bid >> 3;
    const int rb = (xcd & 1) * 8 + (idx >> 2);
    const int ct = (xcd >> 1) * 4 + (idx & 3);
    if (bid == 255 && w == 7) cnt[lane * 16] = 0u;
    const int fb = bid >> 3, eb = bid & 7;
    const bool isd = ct < 6;
    const int c2 = ct - 6;
    const int n0 = 16 * (isd ? (ct < 4 ? 12 * ct : 48 + 8 * (ct - 4)) : (c2 < 8 ? 12 * c2 : 96 + 8 * (c2 - 8)));
    const int nu = isd ? (ct < 4 ? 12 : 8) : (c2 < 8 ? 12 : 8);
    unsigned pf[12] = {0, 0, 0, 0, 0, 0, 0, 0, 0, 0, 0, 0};
    if (w >= 4) {
        if (w == 7) {
            if (bid < 224) {
#pragma unroll
                for (int i = 0; i < 8; ++i)
                    GLDS(w2 + (size_t)(fb * 64 + i * 8 + (lane >> 3)) * D_ + eb * 64 + (lane & 7) * 8, smem + K1_W2 + i * 1024);
            }
            if (!isd && lane < nu * 2) GLDS(b1 + n0 + lane * 8, smem + K1_B1);
            {
                const int sl = xcd >> 1;
                const int nl = sl < 2 ? 23 : 18;
                const int row0 = (xcd & 1) * 1024 + idx * 32;
#pragma unroll
                for (int i = 0; i < 12; ++i) {
                    if ((i % PFG) == 0) {
                        if (i == 0) { for (int q = 0; q < 8 - 12 / PFG; ++q) BAR(); }
                        BAR();
                    }
                    int t = i * 64 + lane;
                    if (t >= 32 * nl || xcd >= 6) t = lane;
                    const int r = t / nl, l = t - r * nl;
                    const int kb = (xcd < 6 ? sl : 0) * 23 + l;
                    const u16* p = (kb < 32 ? Cr + kb * 64 : Ci + (kb - 32) * 64) + (size_t)(row0 + r) * S_;
                    pf[i] = *reinterpret_cast<const unsigned*>(p);
                    __builtin_amdgcn_sched_barrier(0);
                }
            }
            WAITV(12);
        } else {
            if (isd) k1_loader<true>(smem, x, Dr, Di, w1, rb, n0, nu, w - 4, lane);
            else k1_loader<false>(smem, x, Dr, Di, w1, rb, n0, nu, w - 4, lane);
            __builtin_amdgcn_s_setprio(0);
        }
        BAR();
    } else {
        if (isd) k1_body<true>(smem, nu, w, lane);
        else k1_body<false>(smem, nu, w, lane);
    }
    BAR();
    if (isd) k1_out<true>(smem, BT, H, rb, n0, nu, w, lane);
    else k1_out<false>(smem, BT, H, rb, n0, nu, w, lane);
    if (bid < 224) {
        const int fr = lane & 15, fq = lane >> 4, q = fr >> 2, p = fr & 3;
        const int eu = w & 3, fh = w >> 2;
        bf16x4 h[2];
#pragma unroll
        for (int t2 = 0; t2 < 2; ++t2) {
            const int f = fh * 32 + fq * 8 + t2 * 4 + q;
            h[t2] = __builtin_amdgcn_ds_read_tr16_b64_v4i16(
                (__attribute__((address_space(3))) bf16x4*)(smem + K1_W2 + f * 128 + eu * 32 + p * 8));
        }
        bf16x8 o = __builtin_shufflevector(h[0], h[1], 0, 1, 2, 3, 4, 5, 6, 7);
        *reinterpret_cast<bf16x8*>(BT + (size_t)(eb * 64 + eu * 16 + fr) * KT_ + 4096 + fb * 64 + fh * 32 + fq * 8) = o;
    }
#pragma unroll
    for (int i = 0; i < 12; ++i) asm volatile("" :: "v"(pf[i]));
}

__global__ __launch_bounds__(256 + 64 * NLD, 2) void k2(const u16* __restrict__ Cr, const u16* __restrict__ Ci,
                                             const u16* __restrict__ H, const u16* __restrict__ BT,
                                             float* __restrict__ XB, unsigned* __restrict__ cnt,
                                             const u16* __restrict__ x, const u16* __restrict__ b2,
                                             u16* __restrict__ out) {
    extern __shared__ __attribute__((aligned(16))) char smem[];
    const int tid = threadIdx.x, lane = tid & 63, w = __builtin_amdgcn_readfirstlane(tid >> 6);
    const int bid = blockIdx.x, xcd = bid & 7, idx = bid >> 3;
    const int ksl = xcd >> 1;
    const int rb = (xcd & 1) * 8 + (idx >> 2);
    const int nt = idx & 3;
    constexpr int NK = 23;
    if (w >= 4) {
        const bool isA = ((w - 4) < NLD / 2);
        const int part = (w - 4) % (NLD / 2);
        __builtin_amdgcn_s_setprio(LPRIO);
        const uint64_t pA0 = (uint64_t)(Cr + (size_t)(rb * 128) * S_);
        const uint64_t pA1 = (uint64_t)(Ci + (size_t)(rb * 128) * S_) - 32 * 128;
        const uint64_t pA2 = (uint64_t)(H + (size_t)(rb * 128) * F_) - 64 * 128;
        const uint64_t pB = (uint64_t)(BT + (size_t)(nt * 128) * KT_);
        const unsigned r0 = lane >> 3;
        const unsigned c0 = ((lane & 7) ^ ((lane >> 4) & 7)) * 16, c1 = ((lane & 7) ^ ((4 + (lane >> 4)) & 7)) * 16;
#define K2_LOAD(j)                                                                   \
    do {                                                                             \
        const int kb = ksl * NK + (j);                                               \
        char* dst = smem + ((j) & 3) * 32768 + (isA ? 0 : 16384) + part * (32 / NLD) * 1024; \
        uint64_t base = kb < 32 ? pA0 : (kb < 64 ? pA1 : pA2);                       \
        unsigned ldb = kb < 64 ? S_ * 2 : F_ * 2;                                    \
        if (!isA) { base = pB; ldb = KT_ * 2; }                                      \
        base += kb * 128 + (uint64_t)(part * (32 / NLD) * 8) * ldb;                  \
        const unsigned o0 = r0 * ldb + c0, o1 = r0 * ldb + c1;                       \
        _Pragma("unroll") for (int i = 0; i < 32 / NLD; ++i)                         \
            GLDS((const char*)(base + (uint64_t)(i * 8) * ldb) + ((i & 1) ? o1 : o0), dst + i * 1024); \
    } while (0)
        K2_LOAD(0); K2_LOAD(1); K2_LOAD(2);
        if (NLD == 2) WAITV(32); else WAITV(16);
        BAR();
#pragma unroll 1
        for (int j = 0; j < NK - 1; ++j) {
            if (j + 2 < NK) { if (NLD == 2) WAITV(16); else WAITV(8); } else WAITV(0);
            BAR();
            if (j + 3 < NK) K2_LOAD(j + 3);
        }
        return;
    }
    const int fr = lane & 15, fq = lane >> 4;
    const int wm = w >> 1, wn = w & 1;
    f32x4 acc[4][4] = {};
    const unsigned aoff = (wm * 64 + fr) * 128 + ((fq ^ ((fr >> 1) & 7)) * 16);
    const unsigned boff = 16384 + (wn * 64 + fr) * 128 + ((fq ^ ((fr >> 1) & 7)) * 16);
    bf16x8 a0[4], b0[4], a1[4], b1[4];
#define K2_LDF(af, bf, j, kk)                                                                                  \
    do {                                                                                                       \
        const char* sa = smem + ((j) & 3) * 32768;                                                             \
        _Pragma("unroll") for (int m = 0; m < 4; ++m) {                                                        \
            af[m] = *reinterpret_cast<const bf16x8*>(sa + (aoff ^ ((kk) * 64)) + m * 2048);                    \
            bf[m] = *reinterpret_cast<const bf16x8*>(sa + (boff ^ ((kk) * 64)) + m * 2048);                    \
        }                                                                                                      \
    } while (0)
#define K2_MMA(af, bf)                                                               \
    do {                                                                             \
        _Pragma("unroll") for (int m = 0; m < 4; ++m)                                \
            _Pragma("unroll") for (int n = 0; n < 4; ++n) acc[m][n] = MFMA(bf[n], af[m], acc[m][n]); \
    } while (0)
#define K2_SCHED()                                                                   \
    do {                                                                             \
        _Pragma("unroll") for (int q = 0; q < 8; ++q) {                              \
            __builtin_amdgcn_sched_group_barrier(0x100, 1, 0);                       \
            __builtin_amdgcn_sched_group_barrier(0x008, 2, 0);                       \
        }                                                                            \
    } while (0)
    BAR();
    K2_LDF(a0, b0, 0, 0);
#pragma unroll 1
    for (int j = 0; j < NK - 1; ++j) {
        K2_LDF(a1, b1, j, 1);
        K2_MMA(a0, b0);
        K2_SCHED();
        BAR();
        K2_LDF(a0, b0, j + 1, 0);
        K2_MMA(a1, b1);
        K2_SCHED();
    }
    K2_LDF(a1, b1, NK - 1, 1);
    K2_MMA(a0, b0);
    K2_MMA(a1, b1);
    const int tile = rb * 4 + nt;
    const __amdgpu_buffer_rsrc_t xr = __builtin_amdgcn_make_buffer_rsrc((void*)XB, 0, 64 * 262144, 0x00020000);
    const unsigned xo = (unsigned)tile * 262144u + (unsigned)w * 4096u + (unsigned)lane * 16u;
#pragma unroll
    for (int q = 0; q < 4; ++q) {
        if (q != ksl) {
#pragma unroll
            for (int n = 0; n < 4; ++n)
                __builtin_amdgcn_raw_buffer_store_b128(__builtin_bit_cast(u32x4, acc[q][n]), xr, xo + (q * 4 + ksl) * 16384 + n * 1024, 0, 16);
        }
    }
    f32x4 own[4];
#pragma unroll
    for (int n = 0; n < 4; ++n) own[n] = ksl == 0 ? acc[0][n] : (ksl == 1 ? acc[1][n] : (ksl == 2 ? acc[2][n] : acc[3][n]));
    const int row = rb * 128 + wm * 64 + ksl * 16 + fr;
    const int col = nt * 128 + wn * 64 + fq * 4;
    u32x2 xv[4], bv[4];
#pragma unroll
    for (int n = 0; n < 4; ++n) {
        xv[n] = *reinterpret_cast<const u32x2*>(x + (size_t)row * D_ + col + n * 16);
        bv[n] = *reinterpret_cast<const u32x2*>(b2 + col + n * 16);
    }
    WAITV(0);
    BAR();
    if (tid == 0) {
        __hip_atomic_fetch_add(cnt + tile * 16, 1u, __ATOMIC_RELAXED, __HIP_MEMORY_SCOPE_AGENT);
        for (unsigned spin = 0; spin < (1u << 24); ++spin) {
            if (__hip_atomic_load(cnt + tile * 16, __ATOMIC_RELAXED, __HIP_MEMORY_SCOPE_AGENT) >= 4u) break;
            __builtin_amdgcn_s_sleep(1);
        }
    }
    BAR();
    {
        u32x4 pv[3][4];
#pragma unroll
        for (int i = 0; i < 3; ++i) {
            const int p = (ksl + 1 + i) & 3;
#pragma unroll
            for (int n = 0; n < 4; ++n) pv[i][n] = __builtin_amdgcn_raw_buffer_load_b128(xr, xo + (ksl * 4 + p) * 16384 + n * 1024, 0, 16);
        }
#pragma unroll
        for (int i = 0; i < 3; ++i)
#pragma unroll
            for (int n = 0; n < 4; ++n) own[n] += __builtin_bit_cast(f32x4, pv[i][n]);
    }
#pragma unroll
    for (int n = 0; n < 4; ++n) {
        float r[4];
#pragma unroll
        for (int e = 0; e < 4; ++e)
            r[e] = own[n][e] + bf2f((u16)(xv[n][e >> 1] >> ((e & 1) * 16))) + bf2f((u16)(bv[n][e >> 1] >> ((e & 1) * 16)));
        u32x2 o;
        o[0] = pack2(r[0], r[1]);
        o[1] = pack2(r[2], r[3]);
        *reinterpret_cast<u32x2*>(out + (size_t)row * D_ + col + n * 16) = o;
    }
}

extern "C" void kernel_launch(void* const* d_in, const int* in_sizes, int n_in,
                              void* d_out, int out_size, void* d_ws, size_t ws_size,
                              hipStream_t stream) {
    const u16* x = (const u16*)d_in[0];
    const u16* Cr = (const u16*)d_in[1];
    const u16* Ci = (const u16*)d_in[2];
    const u16* Dr = (const u16*)d_in[3];
    const u16* Di = (const u16*)d_in[4];
    const u16* w1 = (const u16*)d_in[6];
    const u16* b1 = (const u16*)d_in[7];
    const u16* w2 = (const u16*)d_in[8];
    const u16* b2 = (const u16*)d_in[9];
    u16* out = (u16*)d_out;
    char* ws = (char*)d_ws;
    u16* BT = (u16*)ws;
    u16* H = (u16*)(ws + 6291456);
    float* XB = (float*)(ws + 6291456 + 7340032);
    unsigned* cnt = (unsigned*)(ws + 6291456 + 7340032 + 16777216);
    (void)hipFuncSetAttribute((const void*)k1, hipFuncAttributeMaxDynamicSharedMemorySize, K1_LDS);
    (void)hipFuncSetAttribute((const void*)k2, hipFuncAttributeMaxDynamicSharedMemorySize, 131072);
    k1<<<256, 512, K1_LDS, stream>>>(x, Dr, Di, w1, b1, w2, BT, H, cnt, Cr, Ci);
    k2<<<256, 256 + 64 * NLD, 131072, stream>>>(Cr, Ci, H, BT, XB, cnt, x, b2, out);
}
```

```cpp
#include <hip/hip_runtime.h>
#include <hip/hip_bf16.h>
#include <stdint.h>

typedef __attribute__((ext_vector_type(8))) short bf16x8;
typedef __attribute__((ext_vector_type(4))) short bf16x4;
typedef __attribute__((ext_vector_type(4))) float f32x4;
typedef __attribute__((ext_vector_type(2))) __bf16 bfx2;
typedef __attribute__((ext_vector_type(4))) unsigned u32x4;
typedef __attribute__((ext_vector_type(2))) unsigned u32x2;
typedef unsigned short u16;
#define LDSP(p) ((__attribute__((address_space(3))) void*)(p))

#define NLD 4
#define LPRIO 3
#define PFG 2
#define STPOL 3
#define S_ 2048
#define D_ 512
#define F_ 1792
#define KT_ 5888

__device__ __forceinline__ float bf2f(u16 v) { return __uint_as_float(((unsigned)v) << 16); }
__device__ __forceinline__ unsigned pack2(float lo, float hi) {
    bfx2 r = {(__bf16)lo, (__bf16)hi};
    return __builtin_bit_cast(unsigned, r);
}
__device__ __forceinline__ float rbf(float f) { return (float)(__bf16)f; }
__device__ __forceinline__ float dot2(unsigned a, unsigned b, float c) {
    return __builtin_amdgcn_fdot2_f32_bf16(__builtin_bit_cast(bfx2, a), __builtin_bit_cast(bfx2, b), c, false);
}
__device__ __forceinline__ bf16x8 sel8(int c, bf16x8 a, bf16x8 b) {
    const u32x4 x = __builtin_bit_cast(u32x4, a), y = __builtin_bit_cast(u32x4, b);
    u32x4 r;
    r[0] = c ? x[0] : y[0]; r[1] = c ? x[1] : y[1]; r[2] = c ? x[2] : y[2]; r[3] = c ? x[3] : y[3];
    return __builtin_bit_cast(bf16x8, r);
}
#define ST16(p, v) asm volatile("global_store_dwordx4 %0, %1, off sc0 sc1\n\ts_nop 1" :: "v"(p), "v"(v) : "memory")
#define WAITV(n) asm volatile("s_waitcnt vmcnt(" #n ")" ::: "memory")
#define BAR() __builtin_amdgcn_s_barrier()
#define GLDS(src, dst) __builtin_amdgcn_global_load_lds((const void*)(src), LDSP(dst), 16, 0, 0)
#define MFMA(a, b, c) __builtin_amdgcn_mfma_f32_16x16x32_bf16(a, b, c, 0, 0, 0)

#define K1_STG 40960
#define K1_STATS (3 * K1_STG)
#define K1_CS (K1_STATS + 1024)
#define K1_B1 (K1_CS + 1024)
#define K1_W2 (K1_B1 + 1024)
#define K1_LDS (K1_W2 + 8192)

template <bool ISD>
__device__ __forceinline__ void k1_loader(char* smem, const u16* __restrict__ x, const u16* __restrict__ Dr,
                                          const u16* __restrict__ Di, const u16* __restrict__ w1,
                                          int rb, int n0, int nu, int L, int lane) {
    __builtin_amdgcn_s_setprio(3);
    const unsigned r0 = lane >> 3, c = lane & 7, hb = (lane >> 4) & 1;
    unsigned so[4];
    uint64_t pLo, pHi;
    int cross = 64;
    if (L == 0) {
#pragma unroll
        for (int q = 0; q < 4; ++q) so[q] = r0 * (D_ * 2) + (c ^ ((q << 1) | hb)) * 16;
        pLo = pHi = (uint64_t)(x + (size_t)rb * 128 * D_);
    } else {
#pragma unroll
        for (int q = 0; q < 4; ++q) {
            if (ISD) so[q] = r0 * (D_ * 2) + (c ^ (((q & 1) * 4 + (lane >> 4)) & 7)) * 16;
            else so[q] = r0 * (F_ * 2) + (c ^ ((hb | ((q & 1) << 1)) << 1)) * 16;
        }
        if (ISD) {
            const int rr = n0 + (L - 1) * 96;
            pLo = (uint64_t)Dr + (uint64_t)rr * (D_ * 2);
            pHi = (uint64_t)Di + (uint64_t)rr * (D_ * 2) - 512 * (D_ * 2);
            cross = (512 - rr) >> 3;
        } else {
            pLo = pHi = (uint64_t)(w1 + n0);
        }
    }
    const int ni = L == 0 ? 16 : (L == 1 ? 12 : 2 * nu - 12);
    char* dbase = smem + (L == 0 ? 0 : (L == 1 ? 16384 : 16384 + 12288));
#define K1_LOAD(j)                                                                                      \
    do {                                                                                                \
        char* dst = dbase + ((j) % 3) * K1_STG;                                                         \
        if (L == 0 || ISD) {                                                                            \
            _Pragma("unroll") for (int i = 0; i < 16; ++i) if (i < ni)                                  \
                GLDS((const char*)((i < cross ? pLo : pHi) + (j) * 128 + i * 8 * (D_ * 2)) + so[i & 3], dst + i * 1024); \
        } else if (L == 1) {                                                                            \
            _Pragma("unroll") for (int i = 0; i < 12; ++i)                                              \
                GLDS((const char*)(pLo + (uint64_t)(j) * 64 * (F_ * 2) + (i & 7) * 8 * (F_ * 2) + (i >> 3) * 128) + so[i & 1], dst + i * 1024); \
        } else {                                                                                        \
            _Pragma("unroll") for (int i = 12; i < 24; ++i) if (i - 12 < ni)                            \
                GLDS((const char*)(pLo + (uint64_t)(j) * 64 * (F_ * 2) + (i & 7) * 8 * (F_ * 2) + (i >> 3) * 128) + so[i & 1], dst + (i - 12) * 1024); \
        }                                                                                               \
    } while (0)
    K1_LOAD(0);
    K1_LOAD(1);
#pragma unroll 1
    for (int j = 0; j < 8; ++j) {
        if (j < 7) { if (ni == 16) WAITV(16); else if (ni == 12) WAITV(12); else WAITV(4); }
        else WAITV(0);
        BAR();
        if (j + 2 < 8) K1_LOAD(j + 2);
    }
#undef K1_LOAD
}

template <bool ISD>
__device__ __forceinline__ void k1_body(char* smem, int nu, int w, int lane) {
    const int fr = lane & 15, fq = lane >> 4;
    const int wm = w >> 1, wn = w & 1;
    const unsigned aoff = (wm * 64 + ((fr >> 2) << 3) + (fr & 3)) * 128 + ((fq ^ ((fr >> 1) & 7)) * 16);
    unsigned boff[2];
#pragma unroll
    for (int e = 0; e < 2; ++e) {
        const int u = wn * 2 + e;
        if (ISD) boff[e] = 16384 + (u * 16 + fr) * 128 + ((fq ^ ((fr >> 1) & 7)) * 16);
        else {
            const int q = fr >> 2, p = fr & 3;
            const int f = ((q >> 1) & 1) | ((fq & 1) << 1);
            boff[e] = 16384 + (fq * 8 + q) * 128 + ((u ^ f) * 32) + p * 8;
        }
    }
    f32x4 acc[4][6] = {};
    const bf16x8 ones = {0x3f80, 0x3f80, 0x3f80, 0x3f80, 0x3f80, 0x3f80, 0x3f80, 0x3f80};
    f32x4 cs[3] = {}, s1[2] = {}, s2[2] = {};
    bf16x8 a0[4], b0[6], a1[4], b1f[6];
#define K1_LDF(af, bfr, j, kk)                                                                                   \
    do {                                                                                                         \
        const char* sb = smem + ((j) % 3) * K1_STG;                                                              \
        _Pragma("unroll") for (int m = 0; m < 4; ++m)                                                            \
            af[m] = *reinterpret_cast<const bf16x8*>(sb + (aoff ^ ((kk) * 64)) + (m >> 1) * 4096 + (m & 1) * 512); \
        _Pragma("unroll") for (int i = 0; i < 6; ++i) {                                                          \
            if (ISD) bfr[i] = *reinterpret_cast<const bf16x8*>(sb + (boff[i & 1] ^ ((kk) * 64)) + (i >> 1) * 8192); \
            else {                                                                                               \
                bf16x4 h0 = __builtin_amdgcn_ds_read_tr16_b64_v4i16((__attribute__((address_space(3))) bf16x4*)(sb + boff[i & 1] + (i >> 1) * 8192 + (kk) * 4096));        \
                bf16x4 h1 = __builtin_amdgcn_ds_read_tr16_b64_v4i16((__attribute__((address_space(3))) bf16x4*)(sb + boff[i & 1] + (i >> 1) * 8192 + (kk) * 4096 + 512));  \
                bfr[i] = __builtin_shufflevector(h0, h1, 0, 1, 2, 3, 4, 5, 6, 7);                                \
            }                                                                                                    \
        }                                                                                                        \
    } while (0)
#define K1_MMA(af, bfr)                                                                                          \
    do {                                                                                                         \
        _Pragma("unroll") for (int i = 0; i < 6; ++i)                                                            \
            _Pragma("unroll") for (int m = 0; m < 4; ++m)                                                        \
                acc[m][i] = ISD ? MFMA(af[m], bfr[i], acc[m][i]) : MFMA(bfr[i], af[m], acc[m][i]);               \
        _Pragma("unroll") for (int t = 0; t < 2; ++t) {                                                          \
            const bf16x8 sa_ = sel8(wn, af[2 + t], af[t]);                                                           \
            s1[t] = MFMA(sa_, ones, s1[t]); s2[t] = MFMA(sa_, sa_, s2[t]);                                       \
        }                                                                                                        \
        _Pragma("unroll") for (int i = 0; i < 3; ++i) {                                                          \
            const bf16x8 sb_ = sel8(wm, bfr[3 + i], bfr[i]);                                                         \
            cs[i] = ISD ? MFMA(ones, sb_, cs[i]) : MFMA(sb_, ones, cs[i]);                                       \
        }                                                                                                        \
        _Pragma("unroll") for (int q = 0; q < 31; ++q) {                                                         \
            __builtin_amdgcn_sched_group_barrier(0x008, 1, 0);                                                   \
            if (q < (ISD ? 10 : 16)) __builtin_amdgcn_sched_group_barrier(0x100, 1, 0);                          \
            if (q < 20) __builtin_amdgcn_sched_group_barrier(0x002, 1, 0);                                       \
        }                                                                                                        \
    } while (0)
    BAR();
    K1_LDF(a0, b0, 0, 0);
#pragma unroll 1
    for (int j = 0; j < 7; ++j) {
        K1_LDF(a1, b1f, j, 1);
        K1_MMA(a0, b0);
        BAR();
        K1_LDF(a0, b0, j + 1, 0);
        K1_MMA(a1, b1f);
    }
    K1_LDF(a1, b1f, 7, 1);
    K1_MMA(a0, b0);
    K1_MMA(a1, b1f);
    BAR();
    float* stats = (float*)(smem + K1_STATS);
    float* csp = (float*)(smem + K1_CS);
    if ((fr >> 2) == fq) {
        const int e = fr & 3;
#pragma unroll
        for (int t = 0; t < 2; ++t) {
            const float a = e == 0 ? s1[t][0] : (e == 1 ? s1[t][1] : (e == 2 ? s1[t][2] : s1[t][3]));
            const float b = e == 0 ? s2[t][0] : (e == 1 ? s2[t][1] : (e == 2 ? s2[t][2] : s2[t][3]));
            const float mean = a * (1.0f / D_);
            const float var = fmaxf(b * (1.0f / D_) - mean * mean, 0.f);
            const float sd = rbf(sqrtf(rbf(rbf(var) + 1e-5f)));
            const int r = wm * 64 + wn * 32 + ((fr >> 2) << 3) + 4 * t + (fr & 3);
            stats[r * 2] = rbf(mean); stats[r * 2 + 1] = 1.0f / sd;
        }
    }
#pragma unroll
    for (int i = 0; i < 3; ++i) {
        const int ii = wm * 3 + i;
        float* d = csp + ((ii >> 1) * 4 + wn * 2 + (ii & 1)) * 16;
        if (ISD) { if (fq == 0) d[fr] = cs[i][0]; }
        else { if (fr == 0) *reinterpret_cast<f32x4*>(d + fq * 4) = cs[i]; }
    }
    {
        char* wb = ISD ? smem + (wn * 32 + fr) * 528 + (wm * 64 + fq * 8) * 4
                       : smem + (wm * 64 + ((fr >> 2) << 3) + (fr & 3)) * 784 + (wn * 32 + fq * 4) * 4;
#pragma unroll
        for (int i = 0; i < 6; ++i)
#pragma unroll
            for (int m = 0; m < 4; ++m) {
                const int uo = (i >> 1) * 4 + (i & 1);
                if (ISD) *reinterpret_cast<f32x4*>(wb + uo * 16 * 528 + (m >> 1) * 128 + (m & 1) * 16) = acc[m][i];
                else *reinterpret_cast<f32x4*>(wb + ((m >> 1) * 32 + (m & 1) * 4) * 784 + uo * 64) = acc[m][i];
            }
    }
    asm volatile("s_waitcnt lgkmcnt(0)" ::: "memory");
}

template <bool ISD>
__device__ __forceinline__ void k1_out(char* smem, u16* __restrict__ BT, u16* __restrict__ H, int rb, int n0, int nu, int w, int lane) {
    const float* stats = (const float*)(smem + K1_STATS);
    const float* csp = (const float*)(smem + K1_CS);
    const int nh = nu >> 1;
    if (ISD) {
        const int c = lane & 15;
        f32x4 st[4];
#pragma unroll
        for (int e = 0; e < 4; ++e) st[e] = *reinterpret_cast<const f32x4*>(stats + c * 16 + e * 4);
#pragma unroll 2
        for (int q = 0; q < nh; ++q) {
            const int e0 = (q * 8 + w) * 4 + (lane >> 4);
            const f32x4 p0 = *reinterpret_cast<const f32x4*>(smem + e0 * 528 + c * 32);
            const f32x4 p1 = *reinterpret_cast<const f32x4*>(smem + e0 * 528 + c * 32 + 16);
            const float cc = csp[e0];
            const int nn = n0 + e0;
            const float sg = nn < 512 ? 1.f : -1.f;
            float v[8];
#pragma unroll
            for (int e = 0; e < 8; ++e) {
                const float mu = st[e >> 1][(e & 1) * 2], ri = st[e >> 1][(e & 1) * 2 + 1];
                v[e] = ((e < 4 ? p0[e & 3] : p1[e & 3]) - mu * cc) * (sg * ri);
            }
            u32x4 o;
#pragma unroll
            for (int e = 0; e < 4; ++e) o[e] = pack2(v[2 * e], v[2 * e + 1]);
            ST16(BT + (size_t)(nn & 511) * KT_ + (nn < 512 ? 0 : 2048) + rb * 128 + c * 8, o);
        }
    } else {
#pragma unroll 2
        for (int q = 0; q < nh; ++q) {
            const int id = q * 8 + (lane >> 3);
            const int ln = nu == 12 ? id % 3 : id & 1;
            const int r = w * 16 + (nu == 12 ? id / 3 : id >> 1);
            const int f0 = ln * 64 + (lane & 7) * 8;
            const f32x4 p0 = *reinterpret_cast<const f32x4*>(smem + r * 784 + f0 * 4);
            const f32x4 p1 = *reinterpret_cast<const f32x4*>(smem + r * 784 + f0 * 4 + 16);
            const f32x4 c0 = *reinterpret_cast<const f32x4*>(csp + f0);
            const f32x4 c1 = *reinterpret_cast<const f32x4*>(csp + f0 + 4);
            const u32x4 bv = *reinterpret_cast<const u32x4*>(smem + K1_B1 + f0 * 2);
            const float mu = stats[r * 2], ri = stats[r * 2 + 1];
            float v[8];
#pragma unroll
            for (int e = 0; e < 8; ++e) {
                const float bb = bf2f((u16)(bv[e >> 1] >> ((e & 1) * 16)));
                float z = ((e < 4 ? p0[e & 3] : p1[e & 3]) - mu * (e < 4 ? c0[e & 3] : c1[e & 3])) * ri + bb;
                z = fmaxf(z, 0.f);
                v[e] = z * z;
            }
            u32x4 o;
#pragma unroll
            for (int e = 0; e < 4; ++e) o[e] = pack2(v[2 * e], v[2 * e + 1]);
            ST16(H + (size_t)(rb * 128 + r) * F_ + n0 + f0, o);
        }
    }
}

__global__ __launch_bounds__(512, 2) void k1(const u16* __restrict__ x, const u16* __restrict__ Dr,
                                             const u16* __restrict__ Di, const u16* __restrict__ w1,
                                             const u16* __restrict__ b1, const u16* __restrict__ w2,
                                             u16* __restrict__ BT, u16* __restrict__ H, unsigned* __restrict__ cnt,
                                             const u16* __restrict__ Cr, const u16* __restrict__ Ci) {
    extern __shared__ __attribute__((aligned(16))) char smem[];
    const int tid = threadIdx.x, lane = tid & 63, w = __builtin_amdgcn_readfirstlane(tid >> 6);
    const int bid = blockIdx.x, xcd = bid & 7, idx = bid >> 3;
    const int rb = (xcd & 1) * 8 + (idx >> 2);
    const int ct = (xcd >> 1) * 4 + (idx & 3);
    if (bid == 255 && w == 7) cnt[lane * 16] = 0u;
    const int fb = bid >> 3, eb = bid & 7;
    const bool isd = ct < 6;
    const int c2 = ct - 6;
    const int n0 = 16 * (isd ? (ct < 4 ? 12 * ct : 48 + 8 * (ct - 4)) : (c2 < 8 ? 12 * c2 : 96 + 8 * (c2 - 8)));
    const int nu = isd ? (ct < 4 ? 12 : 8) : (c2 < 8 ? 12 : 8);
    unsigned pf[12] = {0, 0, 0, 0, 0, 0, 0, 0, 0, 0, 0, 0};
    if (w >= 4) {
        if (w == 7) {
            if (bid < 224) {
#pragma unroll
                for (int i = 0; i < 8; ++i)
                    GLDS(w2 + (size_t)(fb * 64 + i * 8 + (lane >> 3)) * D_ + eb * 64 + (lane & 7) * 8, smem + K1_W2 + i * 1024);
            }
            if (!isd && lane < nu * 2) GLDS(b1 + n0 + lane * 8, smem + K1_B1);
            {
                const int sl = xcd >> 1;
                const int nl = sl < 2 ? 23 : 18;
                const int row0 = (xcd & 1) * 1024 + idx * 32;
#pragma unroll
                for (int i = 0; i < 12; ++i) {
                    if ((i % PFG) == 0) {
                        if (i == 0) { for (int q = 0; q < 8 - 12 / PFG; ++q) BAR(); }
                        BAR();
                    }
                    int t = i * 64 + lane;
                    if (t >= 32 * nl || xcd >= 6) t = lane;
                    const int r = t / nl, l = t - r * nl;
                    const int kb = (xcd < 6 ? sl : 0) * 23 + l;
                    const u16* p = (kb < 32 ? Cr + kb * 64 : Ci + (kb - 32) * 64) + (size_t)(row0 + r) * S_;
                    pf[i] = *reinterpret_cast<const unsigned*>(p);
                    __builtin_amdgcn_sched_barrier(0);
                }
            }
            WAITV(12);
        } else {
            if (isd) k1_loader<true>(smem, x, Dr, Di, w1, rb, n0, nu, w - 4, lane);
            else k1_loader<false>(smem, x, Dr, Di, w1, rb, n0, nu, w - 4, lane);
            __builtin_amdgcn_s_setprio(0);
        }
        BAR();
    } else {
        if (isd) k1_body<true>(smem, nu, w, lane);
        else k1_body<false>(smem, nu, w, lane);
    }
    BAR();
    if (isd) k1_out<true>(smem, BT, H, rb, n0, nu, w, lane);
    else k1_out<false>(smem, BT, H, rb, n0, nu, w, lane);
    if (bid < 224) {
        const int fr = lane & 15, fq = lane >> 4, q = fr >> 2, p = fr & 3;
        const int eu = w & 3, fh = w >> 2;
        bf16x4 h[2];
#pragma unroll
        for (int t2 = 0; t2 < 2; ++t2) {
            const int f = fh * 32 + fq * 8 + t2 * 4 + q;
            h[t2] = __builtin_amdgcn_ds_read_tr16_b64_v4i16(
                (__attribute__((address_space(3))) bf16x4*)(smem + K1_W2 + f * 128 + eu * 32 + p * 8));
        }
        bf16x8 o = __builtin_shufflevector(h[0], h[1], 0, 1, 2, 3, 4, 5, 6, 7);
        *reinterpret_cast<bf16x8*>(BT + (size_t)(eb * 64 + eu * 16 + fr) * KT_ + 4096 + fb * 64 + fh * 32 + fq * 8) = o;
    }
#pragma unroll
    for (int i = 0; i < 12; ++i) asm volatile("" :: "v"(pf[i]));
}

__global__ __launch_bounds__(256 + 64 * NLD, 2) void k2(const u16* __restrict__ Cr, const u16* __restrict__ Ci,
                                             const u16* __restrict__ H, const u16* __restrict__ BT,
                                             float* __restrict__ XB, unsigned* __restrict__ cnt,
                                             const u16* __restrict__ x, const u16* __restrict__ b2,
                                             u16* __restrict__ out) {
    extern __shared__ __attribute__((aligned(16))) char smem[];
    const int tid = threadIdx.x, lane = tid & 63, w = __builtin_amdgcn_readfirstlane(tid >> 6);
    const int bid = blockIdx.x, xcd = bid & 7, idx = bid >> 3;
    const int ksl = xcd >> 1;
    const int rb = (xcd & 1) * 8 + (idx >> 2);
    const int nt = idx & 3;
    constexpr int NK = 23;
    if (w >= 4) {
        const bool isA = ((w - 4) < NLD / 2);
        const int part = (w - 4) % (NLD / 2);
        __builtin_amdgcn_s_setprio(LPRIO);
        const uint64_t pA0 = (uint64_t)(Cr + (size_t)(rb * 128) * S_);
        const uint64_t pA1 = (uint64_t)(Ci + (size_t)(rb * 128) * S_) - 32 * 128;
        const uint64_t pA2 = (uint64_t)(H + (size_t)(rb * 128) * F_) - 64 * 128;
        const uint64_t pB = (uint64_t)(BT + (size_t)(nt * 128) * KT_);
        const unsigned r0 = lane >> 3;
        const unsigned c0 = ((lane & 7) ^ ((lane >> 4) & 7)) * 16, c1 = ((lane & 7) ^ ((4 + (lane >> 4)) & 7)) * 16;
#define K2_LOAD(j)                                                                   \
    do {                                                                             \
        const int kb = ksl * NK + (j);                                               \
        char* dst = smem + ((j) & 3) * 32768 + (isA ? 0 : 16384) + part * (32 / NLD) * 1024; \
        uint64_t base = kb < 32 ? pA0 : (kb < 64 ? pA1 : pA2);                       \
        unsigned ldb = kb < 64 ? S_ * 2 : F_ * 2;                                    \
        if (!isA) { base = pB; ldb = KT_ * 2; }                                      \
        base += kb * 128 + (uint64_t)(part * (32 / NLD) * 8) * ldb;                  \
        const unsigned o0 = r0 * ldb + c0, o1 = r0 * ldb + c1;                       \
        _Pragma("unroll") for (int i = 0; i < 32 / NLD; ++i)                         \
            GLDS((const char*)(base + (uint64_t)(i * 8) * ldb) + ((i & 1) ? o1 : o0), dst + i * 1024); \
    } while (0)
        K2_LOAD(0); K2_LOAD(1); K2_LOAD(2);
        if (NLD == 2) WAITV(32); else WAITV(16);
        BAR();
#pragma unroll 1
        for (int j = 0; j < NK - 1; ++j) {
            if (j + 2 < NK) { if (NLD == 2) WAITV(16); else WAITV(8); } else WAITV(0);
            BAR();
            if (j + 3 < NK) K2_LOAD(j + 3);
        }
        return;
    }
    const int fr = lane & 15, fq = lane >> 4;
    const int wm = w >> 1, wn = w & 1;
    f32x4 acc[4][4] = {};
    const unsigned aoff = (wm * 64 + fr) * 128 + ((fq ^ ((fr >> 1) & 7)) * 16);
    const unsigned boff = 16384 + (wn * 64 + fr) * 128 + ((fq ^ ((fr >> 1) & 7)) * 16);
    bf16x8 a0[4], b0[4], a1[4], b1[4];
#define K2_LDF(af, bf, j, kk)                                                                                  \
    do {                                                                                                       \
        const char* sa = smem + ((j) & 3) * 32768;                                                             \
        _Pragma("unroll") for (int m = 0; m < 4; ++m) {                                                        \
            af[m] = *reinterpret_cast<const bf16x8*>(sa + (aoff ^ ((kk) * 64)) + m * 2048);                    \
            bf[m] = *reinterpret_cast<const bf16x8*>(sa + (boff ^ ((kk) * 64)) + m * 2048);                    \
        }                                                                                                      \
    } while (0)
#define K2_MMA(af, bf)                                                               \
    do {                                                                             \
        _Pragma("unroll") for (int m = 0; m < 4; ++m)                                \
            _Pragma("unroll") for (int n = 0; n < 4; ++n) acc[m][n] = MFMA(bf[n], af[m], acc[m][n]); \
    } while (0)
#define K2_SCHED()                                                                   \
    do {                                                                             \
        _Pragma("unroll") for (int q = 0; q < 8; ++q) {                              \
            __builtin_amdgcn_sched_group_barrier(0x100, 1, 0);                       \
            __builtin_amdgcn_sched_group_barrier(0x008, 2, 0);                       \
        }                                                                            \
    } while (0)
    const int row = rb * 128 + wm * 64 + ksl * 16 + fr;
    const int col = nt * 128 + wn * 64 + fq * 4;
    u32x2 xv[4], bv[4];
#pragma unroll
    for (int n = 0; n < 4; ++n) {
        xv[n] = *reinterpret_cast<const u32x2*>(x + (size_t)row * D_ + col + n * 16);
        bv[n] = *reinterpret_cast<const u32x2*>(b2 + col + n * 16);
    }
    BAR();
    K2_LDF(a0, b0, 0, 0);
#pragma unroll 1
    for (int j = 0; j < NK - 1; ++j) {
        K2_LDF(a1, b1, j, 1);
        K2_MMA(a0, b0);
        K2_SCHED();
        BAR();
        K2_LDF(a0, b0, j + 1, 0);
        K2_MMA(a1, b1);
        K2_SCHED();
    }
    K2_LDF(a1, b1, NK - 1, 1);
    K2_MMA(a0, b0);
    K2_MMA(a1, b1);
    const int tile = rb * 4 + nt;
    const __amdgpu_buffer_rsrc_t xr = __builtin_amdgcn_make_buffer_rsrc((void*)XB, 0, 64 * 131072, 0x00020000);
    const unsigned xo = (unsigned)tile * 131072u + (unsigned)w * 2048u + (unsigned)lane * 16u;
#pragma unroll
    for (int q = 0; q < 4; ++q) {
        if (q != ksl) {
#pragma unroll
            for (int h = 0; h < 2; ++h) {
                u32x4 v;
                v[0] = pack2(acc[q][2 * h][0], acc[q][2 * h][1]); v[1] = pack2(acc[q][2 * h][2], acc[q][2 * h][3]);
                v[2] = pack2(acc[q][2 * h + 1][0], acc[q][2 * h + 1][1]); v[3] = pack2(acc[q][2 * h + 1][2], acc[q][2 * h + 1][3]);
                __builtin_amdgcn_raw_buffer_store_b128(v, xr, xo + (q * 4 + ksl) * 8192 + h * 1024, 0, 16);
            }
        }
    }
    f32x4 own[4];
#pragma unroll
    for (int n = 0; n < 4; ++n) own[n] = ksl == 0 ? acc[0][n] : (ksl == 1 ? acc[1][n] : (ksl == 2 ? acc[2][n] : acc[3][n]));
    WAITV(0);
    BAR();
    if (tid == 0) {
        __hip_atomic_fetch_add(cnt + tile * 16, 1u, __ATOMIC_RELAXED, __HIP_MEMORY_SCOPE_AGENT);
        for (unsigned spin = 0; spin < (1u << 24); ++spin) {
            if (__hip_atomic_load(cnt + tile * 16, __ATOMIC_RELAXED, __HIP_MEMORY_SCOPE_AGENT) >= 4u) break;
            __builtin_amdgcn_s_sleep(1);
        }
    }
    BAR();
    {
        u32x4 pv[3][2];
#pragma unroll
        for (int i = 0; i < 3; ++i) {
            const int p = (ksl + 1 + i) & 3;
#pragma unroll
            for (int h = 0; h < 2; ++h) pv[i][h] = __builtin_amdgcn_raw_buffer_load_b128(xr, xo + (ksl * 4 + p) * 8192 + h * 1024, 0, 16);
        }
#pragma unroll
        for (int i = 0; i < 3; ++i)
#pragma unroll
            for (int n = 0; n < 4; ++n)
#pragma unroll
                for (int e = 0; e < 4; ++e) {
                    const unsigned u = pv[i][n >> 1][(n & 1) * 2 + (e >> 1)];
                    own[n][e] += __uint_as_float((e & 1) ? (u & 0xffff0000u) : (u << 16));
                }
    }
#pragma unroll
    for (int n = 0; n < 4; ++n) {
        float r[4];
#pragma unroll
        for (int e = 0; e < 4; ++e)
            r[e] = own[n][e] + bf2f((u16)(xv[n][e >> 1] >> ((e & 1) * 16))) + bf2f((u16)(bv[n][e >> 1] >> ((e & 1) * 16)));
        u32x2 o;
        o[0] = pack2(r[0], r[1]);
        o[1] = pack2(r[2], r[3]);
        *reinterpret_cast<u32x2*>(out + (size_t)row * D_ + col + n * 16) = o;
    }
}

extern "C" void kernel_launch(void* const* d_in, const int* in_sizes, int n_in,
                              void* d_out, int out_size, void* d_ws, size_t ws_size,
                              hipStream_t stream) {
    const u16* x = (const u16*)d_in[0];
    const u16* Cr = (const u16*)d_in[1];
    const u16* Ci = (const u16*)d_in[2];
    const u16* Dr = (const u16*)d_in[3];
    const u16* Di = (const u16*)d_in[4];
    const u16* w1 = (const u16*)d_in[6];
    const u16* b1 = (const u16*)d_in[7];
    const u16* w2 = (const u16*)d_in[8];
    const u16* b2 = (const u16*)d_in[9];
    u16* out = (u16*)d_out;
    char* ws = (char*)d_ws;
    u16* BT = (u16*)ws;
    u16* H = (u16*)(ws + 6291456);
    float* XB = (float*)(ws + 6291456 + 7340032);
    unsigned* cnt = (unsigned*)(ws + 6291456 + 7340032 + 16777216);
    (void)hipFuncSetAttribute((const void*)k1, hipFuncAttributeMaxDynamicSharedMemorySize, K1_LDS);
    (void)hipFuncSetAttribute((const void*)k2, hipFuncAttributeMaxDynamicSharedMemorySize, 131072);
    k1<<<256, 512, K1_LDS, stream>>>(x, Dr, Di, w1, b1, w2, BT, H, cnt, Cr, Ci);
    k2<<<256, 256 + 64 * NLD, 131072, stream>>>(Cr, Ci, H, BT, XB, cnt, x, b2, out);
}
```

```cpp
#include <hip/hip_runtime.h>
#include <hip/hip_bf16.h>
#include <stdint.h>

typedef __attribute__((ext_vector_type(8))) short bf16x8;
typedef __attribute__((ext_vector_type(4))) short bf16x4;
typedef __attribute__((ext_vector_type(4))) float f32x4;
typedef __attribute__((ext_vector_type(2))) __bf16 bfx2;
typedef __attribute__((ext_vector_type(4))) unsigned u32x4;
typedef __attribute__((ext_vector_type(2))) unsigned u32x2;
typedef unsigned short u16;
#define LDSP(p) ((__attribute__((address_space(3))) void*)(p))

#define NLD 4
#define LPRIO 3
#define PFG 2
#define STPOL 3
#define S_ 2048
#define D_ 512
#define F_ 1792
#define KT_ 5888

__device__ __forceinline__ float bf2f(u16 v) { return __uint_as_float(((unsigned)v) << 16); }
__device__ __forceinline__ unsigned pack2(float lo, float hi) {
    bfx2 r = {(__bf16)lo, (__bf16)hi};
    return __builtin_bit_cast(unsigned, r);
}
__device__ __forceinline__ float rbf(float f) { return (float)(__bf16)f; }
__device__ __forceinline__ float dot2(unsigned a, unsigned b, float c) {
    return __builtin_amdgcn_fdot2_f32_bf16(__builtin_bit_cast(bfx2, a), __builtin_bit_cast(bfx2, b), c, false);
}
__device__ __forceinline__ bf16x8 sel8(int c, bf16x8 a, bf16x8 b) {
    const u32x4 x = __builtin_bit_cast(u32x4, a), y = __builtin_bit_cast(u32x4, b);
    u32x4 r;
    r[0] = c ? x[0] : y[0]; r[1] = c ? x[1] : y[1]; r[2] = c ? x[2] : y[2]; r[3] = c ? x[3] : y[3];
    return __builtin_bit_cast(bf16x8, r);
}
#define ST16(p, v) asm volatile("global_store_dwordx4 %0, %1, off sc0 sc1\n\ts_nop 1" :: "v"(p), "v"(v) : "memory")
#define WAITV(n) asm volatile("s_waitcnt vmcnt(" #n ")" ::: "memory")
#define BAR() __builtin_amdgcn_s_barrier()
#define GLDS(src, dst) __builtin_amdgcn_global_load_lds((const void*)(src), LDSP(dst), 16, 0, 0)
#define MFMA(a, b, c) __builtin_amdgcn_mfma_f32_16x16x32_bf16(a, b, c, 0, 0, 0)

#define K1_STG 40960
#define K1_STATS (3 * K1_STG)
#define K1_CS (K1_STATS + 1024)
#define K1_B1 (K1_CS + 1024)
#define K1_W2 (K1_B1 + 1024)
#define K1_LDS (K1_W2 + 8192)

__device__ __forceinline__ void k1_loader(char* smem, const u16* __restrict__ x, const u16* __restrict__ Dr,
                                          const u16* __restrict__ Di, const u16* __restrict__ w1,
                                          bool isd, int rb, int n0, int nu, int L, int lane) {
    __builtin_amdgcn_s_setprio(3);
    const unsigned r0 = lane >> 3, c = lane & 7, hb = (lane >> 4) & 1;
    const bool wt = (L != 0) && !isd;
    unsigned so[4];
#pragma unroll
    for (int q = 0; q < 4; ++q) {
        const unsigned sw = L == 0 ? ((q << 1) | hb) : (isd ? (((q & 1) * 4 + (lane >> 4)) & 7) : ((hb | ((q & 1) << 1)) << 1));
        so[q] = r0 * (wt ? F_ * 2 : D_ * 2) + (c ^ sw) * 16;
    }
    const int rr = n0 + (L - 1) * 96;
    uint64_t pLo, pHi;
    int cross = 64;
    if (L == 0) pLo = pHi = (uint64_t)(x + (size_t)rb * 128 * D_);
    else if (isd) {
        pLo = (uint64_t)Dr + (uint64_t)rr * (D_ * 2);
        pHi = (uint64_t)Di + (uint64_t)rr * (D_ * 2) - 512 * (D_ * 2);
        cross = (512 - rr) >> 5;
    } else pLo = pHi = (uint64_t)(w1 + n0);
    const int ng = L == 0 ? 4 : (L == 1 ? 3 : (nu == 12 ? 3 : 1));
    const int g0 = L == 2 ? 3 : 0;
    const unsigned es = wt ? 8 * F_ * 2 : 8 * D_ * 2;
    const unsigned js = wt ? 64 * F_ * 2 : 128;
    char* dbase = smem + (L == 0 ? 0 : (L == 1 ? 16384 : 16384 + 12288));
    int buf = 0;
#pragma unroll 1
    for (int j = -2; j < 8; ++j) {
        if (j >= 0) {
            if (j == 7) WAITV(0); else if (ng == 4) WAITV(16); else if (ng == 3) WAITV(12); else WAITV(4);
            BAR();
        }
        if (j + 2 < 8) {
            char* dst = dbase + buf * K1_STG;
            buf = buf == 2 ? 0 : buf + 1;
#pragma unroll 1
            for (int g = 0; g < ng; ++g) {
                const int G = g + g0;
                const uint64_t b = (g < cross ? pLo : pHi) + (uint64_t)(j + 2) * js +
                                   (wt ? (unsigned)((G & 1) * 4 * (8 * F_ * 2) + (G >> 1) * 128) : (unsigned)(g * 32 * (D_ * 2)));
#pragma unroll
                for (int e = 0; e < 4; ++e) GLDS((const char*)(b + e * es) + so[e], dst + (g * 4 + e) * 1024);
            }
        }
    }
}

template <bool ISD>
__device__ __forceinline__ void k1_body(char* smem, int nu, int w, int lane) {
    const int fr = lane & 15, fq = lane >> 4;
    const int wm = w >> 1, wn = w & 1;
    const unsigned aoff = (wm * 64 + ((fr >> 2) << 3) + (fr & 3)) * 128 + ((fq ^ ((fr >> 1) & 7)) * 16);
    unsigned boff[2];
#pragma unroll
    for (int e = 0; e < 2; ++e) {
        const int u = wn * 2 + e;
        if (ISD) boff[e] = 16384 + (u * 16 + fr) * 128 + ((fq ^ ((fr >> 1) & 7)) * 16);
        else {
            const int q = fr >> 2, p = fr & 3;
            const int f = ((q >> 1) & 1) | ((fq & 1) << 1);
            boff[e] = 16384 + (fq * 8 + q) * 128 + ((u ^ f) * 32) + p * 8;
        }
    }
    f32x4 acc[4][6] = {};
    const bf16x8 ones = {0x3f80, 0x3f80, 0x3f80, 0x3f80, 0x3f80, 0x3f80, 0x3f80, 0x3f80};
    f32x4 cs[3] = {}, s1[2] = {}, s2[2] = {};
    bf16x8 a0[4], b0[6], a1[4], b1f[6];
#define K1_LDF(af, bfr, j, kk)                                                                                   \
    do {                                                                                                         \
        const char* sb = smem + ((j) % 3) * K1_STG;                                                              \
        _Pragma("unroll") for (int m = 0; m < 4; ++m)                                                            \
            af[m] = *reinterpret_cast<const bf16x8*>(sb + (aoff ^ ((kk) * 64)) + (m >> 1) * 4096 + (m & 1) * 512); \
        _Pragma("unroll") for (int i = 0; i < 6; ++i) {                                                          \
            if (ISD) bfr[i] = *reinterpret_cast<const bf16x8*>(sb + (boff[i & 1] ^ ((kk) * 64)) + (i >> 1) * 8192); \
            else {                                                                                               \
                bf16x4 h0 = __builtin_amdgcn_ds_read_tr16_b64_v4i16((__attribute__((address_space(3))) bf16x4*)(sb + boff[i & 1] + (i >> 1) * 8192 + (kk) * 4096));        \
                bf16x4 h1 = __builtin_amdgcn_ds_read_tr16_b64_v4i16((__attribute__((address_space(3))) bf16x4*)(sb + boff[i & 1] + (i >> 1) * 8192 + (kk) * 4096 + 512));  \
                bfr[i] = __builtin_shufflevector(h0, h1, 0, 1, 2, 3, 4, 5, 6, 7);                                \
            }                                                                                                    \
        }                                                                                                        \
    } while (0)
#define K1_MMA(af, bfr)                                                                                          \
    do {                                                                                                         \
        _Pragma("unroll") for (int i = 0; i < 6; ++i)                                                            \
            _Pragma("unroll") for (int m = 0; m < 4; ++m)                                                        \
                acc[m][i] = ISD ? MFMA(af[m], bfr[i], acc[m][i]) : MFMA(bfr[i], af[m], acc[m][i]);               \
        _Pragma("unroll") for (int t = 0; t < 2; ++t) {                                                          \
            const bf16x8 sa_ = sel8(wn, af[2 + t], af[t]);                                                           \
            s1[t] = MFMA(sa_, ones, s1[t]); s2[t] = MFMA(sa_, sa_, s2[t]);                                       \
        }                                                                                                        \
        _Pragma("unroll") for (int i = 0; i < 3; ++i) {                                                          \
            const bf16x8 sb_ = sel8(wm, bfr[3 + i], bfr[i]);                                                         \
            cs[i] = ISD ? MFMA(ones, sb_, cs[i]) : MFMA(sb_, ones, cs[i]);                                       \
        }                                                                                                        \
        _Pragma("unroll") for (int q = 0; q < 31; ++q) {                                                         \
            __builtin_amdgcn_sched_group_barrier(0x008, 1, 0);                                                   \
            if (q < (ISD ? 10 : 16)) __builtin_amdgcn_sched_group_barrier(0x100, 1, 0);                          \
            if (q < 20) __builtin_amdgcn_sched_group_barrier(0x002, 1, 0);                                       \
        }                                                                                                        \
    } while (0)
    BAR();
    K1_LDF(a0, b0, 0, 0);
#pragma unroll 1
    for (int j = 0; j < 7; ++j) {
        K1_LDF(a1, b1f, j, 1);
        K1_MMA(a0, b0);
        BAR();
        K1_LDF(a0, b0, j + 1, 0);
        K1_MMA(a1, b1f);
    }
    K1_LDF(a1, b1f, 7, 1);
    K1_MMA(a0, b0);
    K1_MMA(a1, b1f);
    BAR();
    float* stats = (float*)(smem + K1_STATS);
    float* csp = (float*)(smem + K1_CS);
    if ((fr >> 2) == fq) {
        const int e = fr & 3;
#pragma unroll
        for (int t = 0; t < 2; ++t) {
            const float a = e == 0 ? s1[t][0] : (e == 1 ? s1[t][1] : (e == 2 ? s1[t][2] : s1[t][3]));
            const float b = e == 0 ? s2[t][0] : (e == 1 ? s2[t][1] : (e == 2 ? s2[t][2] : s2[t][3]));
            const float mean = a * (1.0f / D_);
            const float var = fmaxf(b * (1.0f / D_) - mean * mean, 0.f);
            const float sd = rbf(sqrtf(rbf(rbf(var) + 1e-5f)));
            const int r = wm * 64 + wn * 32 + ((fr >> 2) << 3) + 4 * t + (fr & 3);
            stats[r * 2] = rbf(mean); stats[r * 2 + 1] = 1.0f / sd;
        }
    }
#pragma unroll
    for (int i = 0; i < 3; ++i) {
        const int ii = wm * 3 + i;
        float* d = csp + ((ii >> 1) * 4 + wn * 2 + (ii & 1)) * 16;
        if (ISD) { if (fq == 0) d[fr] = cs[i][0]; }
        else { if (fr == 0) *reinterpret_cast<f32x4*>(d + fq * 4) = cs[i]; }
    }
    {
        char* wb = ISD ? smem + (wn * 32 + fr) * 528 + (wm * 64 + fq * 8) * 4
                       : smem + (wm * 64 + ((fr >> 2) << 3) + (fr & 3)) * 784 + (wn * 32 + fq * 4) * 4;
#pragma unroll
        for (int i = 0; i < 6; ++i)
#pragma unroll
            for (int m = 0; m < 4; ++m) {
                const int uo = (i >> 1) * 4 + (i & 1);
                if (ISD) *reinterpret_cast<f32x4*>(wb + uo * 16 * 528 + (m >> 1) * 128 + (m & 1) * 16) = acc[m][i];
                else *reinterpret_cast<f32x4*>(wb + ((m >> 1) * 32 + (m & 1) * 4) * 784 + uo * 64) = acc[m][i];
            }
    }
    asm volatile("s_waitcnt lgkmcnt(0)" ::: "memory");
}

template <bool ISD>
__device__ __forceinline__ void k1_out(char* smem, u16* __restrict__ BT, u16* __restrict__ H, int rb, int n0, int nu, int w, int lane) {
    const float* stats = (const float*)(smem + K1_STATS);
    const float* csp = (const float*)(smem + K1_CS);
    const int nh = nu >> 1;
    if (ISD) {
        const int c = lane & 15;
        f32x4 st[4];
#pragma unroll
        for (int e = 0; e < 4; ++e) st[e] = *reinterpret_cast<const f32x4*>(stats + c * 16 + e * 4);
#pragma unroll 2
        for (int q = 0; q < nh; ++q) {
            const int e0 = (q * 8 + w) * 4 + (lane >> 4);
            const f32x4 p0 = *reinterpret_cast<const f32x4*>(smem + e0 * 528 + c * 32);
            const f32x4 p1 = *reinterpret_cast<const f32x4*>(smem + e0 * 528 + c * 32 + 16);
            const float cc = csp[e0];
            const int nn = n0 + e0;
            const float sg = nn < 512 ? 1.f : -1.f;
            float v[8];
#pragma unroll
            for (int e = 0; e < 8; ++e) {
                const float mu = st[e >> 1][(e & 1) * 2], ri = st[e >> 1][(e & 1) * 2 + 1];
                v[e] = ((e < 4 ? p0[e & 3] : p1[e & 3]) - mu * cc) * (sg * ri);
            }
            u32x4 o;
#pragma unroll
            for (int e = 0; e < 4; ++e) o[e] = pack2(v[2 * e], v[2 * e + 1]);
            ST16(BT + (size_t)(nn & 511) * KT_ + (nn < 512 ? 0 : 2048) + rb * 128 + c * 8, o);
        }
    } else {
#pragma unroll 2
        for (int q = 0; q < nh; ++q) {
            const int id = q * 8 + (lane >> 3);
            const int ln = nu == 12 ? id % 3 : id & 1;
            const int r = w * 16 + (nu == 12 ? id / 3 : id >> 1);
            const int f0 = ln * 64 + (lane & 7) * 8;
            const f32x4 p0 = *reinterpret_cast<const f32x4*>(smem + r * 784 + f0 * 4);
            const f32x4 p1 = *reinterpret_cast<const f32x4*>(smem + r * 784 + f0 * 4 + 16);
            const f32x4 c0 = *reinterpret_cast<const f32x4*>(csp + f0);
            const f32x4 c1 = *reinterpret_cast<const f32x4*>(csp + f0 + 4);
            const u32x4 bv = *reinterpret_cast<const u32x4*>(smem + K1_B1 + f0 * 2);
            const float mu = stats[r * 2], ri = stats[r * 2 + 1];
            float v[8];
#pragma unroll
            for (int e = 0; e < 8; ++e) {
                const float bb = bf2f((u16)(bv[e >> 1] >> ((e & 1) * 16)));
                float z = ((e < 4 ? p0[e & 3] : p1[e & 3]) - mu * (e < 4 ? c0[e & 3] : c1[e & 3])) * ri + bb;
                z = fmaxf(z, 0.f);
                v[e] = z * z;
            }
            u32x4 o;
#pragma unroll
            for (int e = 0; e < 4; ++e) o[e] = pack2(v[2 * e], v[2 * e + 1]);
            ST16(H + (size_t)(rb * 128 + r) * F_ + n0 + f0, o);
        }
    }
}

__global__ __launch_bounds__(512, 2) void k1(const u16* __restrict__ x, const u16* __restrict__ Dr,
                                             const u16* __restrict__ Di, const u16* __restrict__ w1,
                                             const u16* __restrict__ b1, const u16* __restrict__ w2,
                                             u16* __restrict__ BT, u16* __restrict__ H, unsigned* __restrict__ cnt,
                                             const u16* __restrict__ Cr, const u16* __restrict__ Ci) {
    extern __shared__ __attribute__((aligned(16))) char smem[];
    const int tid = threadIdx.x, lane = tid & 63, w = __builtin_amdgcn_readfirstlane(tid >> 6);
    const int bid = blockIdx.x, xcd = bid & 7, idx = bid >> 3;
    const int rb = (xcd & 1) * 8 + (idx >> 2);
    const int ct = (xcd >> 1) * 4 + (idx & 3);
    if (bid == 255 && w == 7) cnt[lane * 16] = 0u;
    const int fb = bid >> 3, eb = bid & 7;
    const bool isd = ct < 6;
    const int c2 = ct - 6;
    const int n0 = 16 * (isd ? (ct < 4 ? 12 * ct : 48 + 8 * (ct - 4)) : (c2 < 8 ? 12 * c2 : 96 + 8 * (c2 - 8)));
    const int nu = isd ? (ct < 4 ? 12 : 8) : (c2 < 8 ? 12 : 8);
    unsigned pf[12] = {0, 0, 0, 0, 0, 0, 0, 0, 0, 0, 0, 0};
    if (w >= 4) {
        if (w == 7) {
            if (bid < 224) {
#pragma unroll
                for (int i = 0; i < 8; ++i)
                    GLDS(w2 + (size_t)(fb * 64 + i * 8 + (lane >> 3)) * D_ + eb * 64 + (lane & 7) * 8, smem + K1_W2 + i * 1024);
            }
            if (!isd && lane < nu * 2) GLDS(b1 + n0 + lane * 8, smem + K1_B1);
            {
                const int sl = xcd >> 1;
                const int nl = sl < 2 ? 23 : 18;
                const int row0 = (xcd & 1) * 1024 + idx * 32;
#pragma unroll
                for (int i = 0; i < 12; ++i) {
                    if ((i % PFG) == 0) {
                        if (i == 0) { for (int q = 0; q < 8 - 12 / PFG; ++q) BAR(); }
                        BAR();
                    }
                    int t = i * 64 + lane;
                    if (t >= 32 * nl || xcd >= 6) t = lane;
                    const int r = t / nl, l = t - r * nl;
                    const int kb = (xcd < 6 ? sl : 0) * 23 + l;
                    const u16* p = (kb < 32 ? Cr + kb * 64 : Ci + (kb - 32) * 64) + (size_t)(row0 + r) * S_;
                    pf[i] = *reinterpret_cast<const unsigned*>(p);
                    __builtin_amdgcn_sched_barrier(0);
                }
            }
            WAITV(12);
        } else {
            k1_loader(smem, x, Dr, Di, w1, isd, rb, n0, nu, w - 4, lane);
            __builtin_amdgcn_s_setprio(0);
        }
        BAR();
    } else {
        if (isd) k1_body<true>(smem, nu, w, lane);
        else k1_body<false>(smem, nu, w, lane);
    }
    BAR();
    if (isd) k1_out<true>(smem, BT, H, rb, n0, nu, w, lane);
    else k1_out<false>(smem, BT, H, rb, n0, nu, w, lane);
    if (bid < 224) {
        const int fr = lane & 15, fq = lane >> 4, q = fr >> 2, p = fr & 3;
        const int eu = w & 3, fh = w >> 2;
        bf16x4 h[2];
#pragma unroll
        for (int t2 = 0; t2 < 2; ++t2) {
            const int f = fh * 32 + fq * 8 + t2 * 4 + q;
            h[t2] = __builtin_amdgcn_ds_read_tr16_b64_v4i16(
                (__attribute__((address_space(3))) bf16x4*)(smem + K1_W2 + f * 128 + eu * 32 + p * 8));
        }
        bf16x8 o = __builtin_shufflevector(h[0], h[1], 0, 1, 2, 3, 4, 5, 6, 7);
        *reinterpret_cast<bf16x8*>(BT + (size_t)(eb * 64 + eu * 16 + fr) * KT_ + 4096 + fb * 64 + fh * 32 + fq * 8) = o;
    }
#pragma unroll
    for (int i = 0; i < 12; ++i) asm volatile("" :: "v"(pf[i]));
}

__global__ __launch_bounds__(256 + 64 * NLD, 2) void k2(const u16* __restrict__ Cr, const u16* __restrict__ Ci,
                                             const u16* __restrict__ H, const u16* __restrict__ BT,
                                             float* __restrict__ XB, unsigned* __restrict__ cnt,
                                             const u16* __restrict__ x, const u16* __restrict__ b2,
                                             u16* __restrict__ out) {
    extern __shared__ __attribute__((aligned(16))) char smem[];
    const int tid = threadIdx.x, lane = tid & 63, w = __builtin_amdgcn_readfirstlane(tid >> 6);
    const int bid = blockIdx.x, xcd = bid & 7, idx = bid >> 3;
    const int ksl = xcd >> 1;
    const int rb = (xcd & 1) * 8 + (idx >> 2);
    const int nt = idx & 3;
    constexpr int NK = 23;
    if (w >= 4) {
        const bool isA = ((w - 4) < NLD / 2);
        const int part = (w - 4) % (NLD / 2);
        __builtin_amdgcn_s_setprio(LPRIO);
        const uint64_t pA0 = (uint64_t)(Cr + (size_t)(rb * 128) * S_);
        const uint64_t pA1 = (uint64_t)(Ci + (size_t)(rb * 128) * S_) - 32 * 128;
        const uint64_t pA2 = (uint64_t)(H + (size_t)(rb * 128) * F_) - 64 * 128;
        const uint64_t pB = (uint64_t)(BT + (size_t)(nt * 128) * KT_);
        const unsigned r0 = lane >> 3;
        const unsigned c0 = ((lane & 7) ^ ((lane >> 4) & 7)) * 16, c1 = ((lane & 7) ^ ((4 + (lane >> 4)) & 7)) * 16;
#define K2_LOAD(j)                                                                   \
    do {                                                                             \
        const int kb = ksl * NK + (j);                                               \
        char* dst = smem + ((j) & 3) * 32768 + (isA ? 0 : 16384) + part * (32 / NLD) * 1024; \
        uint64_t base = kb < 32 ? pA0 : (kb < 64 ? pA1 : pA2);                       \
        unsigned ldb = kb < 64 ? S_ * 2 : F_ * 2;                                    \
        if (!isA) { base = pB; ldb = KT_ * 2; }                                      \
        base += kb * 128 + (uint64_t)(part * (32 / NLD) * 8) * ldb;                  \
        const unsigned o0 = r0 * ldb + c0, o1 = r0 * ldb + c1;                       \
        _Pragma("unroll") for (int i = 0; i < 32 / NLD; ++i)                         \
            GLDS((const char*)(base + (uint64_t)(i * 8) * ldb) + ((i & 1) ? o1 : o0), dst + i * 1024); \
    } while (0)
        K2_LOAD(0); K2_LOAD(1); K2_LOAD(2);
        if (NLD == 2) WAITV(32); else WAITV(16);
        BAR();
#pragma unroll 1
        for (int j = 0; j < NK - 1; ++j) {
            if (j + 2 < NK) { if (NLD == 2) WAITV(16); else WAITV(8); } else WAITV(0);
            BAR();
            if (j + 3 < NK) K2_LOAD(j + 3);
        }
        return;
    }
    const int fr = lane & 15, fq = lane >> 4;
    const int wm = w >> 1, wn = w & 1;
    f32x4 acc[4][4] = {};
    const unsigned aoff = (wm * 64 + fr) * 128 + ((fq ^ ((fr >> 1) & 7)) * 16);
    const unsigned boff = 16384 + (wn * 64 + fr) * 128 + ((fq ^ ((fr >> 1) & 7)) * 16);
    bf16x8 a0[4], b0[4], a1[4], b1[4];
#define K2_LDF(af, bf, j, kk)                                                                                  \
    do {                                                                                                       \
        const char* sa = smem + ((j) & 3) * 32768;                                                             \
        _Pragma("unroll") for (int m = 0; m < 4; ++m) {                                                        \
            af[m] = *reinterpret_cast<const bf16x8*>(sa + (aoff ^ ((kk) * 64)) + m * 2048);                    \
            bf[m] = *reinterpret_cast<const bf16x8*>(sa + (boff ^ ((kk) * 64)) + m * 2048);                    \
        }                                                                                                      \
    } while (0)
#define K2_MMA(af, bf)                                                               \
    do {                                                                             \
        _Pragma("unroll") for (int m = 0; m < 4; ++m)                                \
            _Pragma("unroll") for (int n = 0; n < 4; ++n) acc[m][n] = MFMA(bf[n], af[m], acc[m][n]); \
    } while (0)
#define K2_SCHED()                                                                   \
    do {                                                                             \
        _Pragma("unroll") for (int q = 0; q < 8; ++q) {                              \
            __builtin_amdgcn_sched_group_barrier(0x100, 1, 0);                       \
            __builtin_amdgcn_sched_group_barrier(0x008, 2, 0);                       \
        }                                                                            \
    } while (0)
    const int row = rb * 128 + wm * 64 + ksl * 16 + fr;
    const int col = nt * 128 + wn * 64 + fq * 4;
    u32x2 xv[4], bv[4];
#pragma unroll
    for (int n = 0; n < 4; ++n) {
        xv[n] = *reinterpret_cast<const u32x2*>(x + (size_t)row * D_ + col + n * 16);
        bv[n] = *reinterpret_cast<const u32x2*>(b2 + col + n * 16);
    }
    BAR();
    K2_LDF(a0, b0, 0, 0);
#pragma unroll 1
    for (int j = 0; j < NK - 1; ++j) {
        K2_LDF(a1, b1, j, 1);
        K2_MMA(a0, b0);
        K2_SCHED();
        BAR();
        K2_LDF(a0, b0, j + 1, 0);
        K2_MMA(a1, b1);
        K2_SCHED();
    }
    K2_LDF(a1, b1, NK - 1, 1);
    K2_MMA(a0, b0);
    K2_MMA(a1, b1);
    const int tile = rb * 4 + nt;
    const __amdgpu_buffer_rsrc_t xr = __builtin_amdgcn_make_buffer_rsrc((void*)XB, 0, 64 * 131072, 0x00020000);
    const unsigned xo = (unsigned)tile * 131072u + (unsigned)w * 2048u + (unsigned)lane * 16u;
#pragma unroll
    for (int q = 0; q < 4; ++q) {
        if (q != ksl) {
#pragma unroll
            for (int h = 0; h < 2; ++h) {
                u32x4 v;
                v[0] = pack2(acc[q][2 * h][0], acc[q][2 * h][1]); v[1] = pack2(acc[q][2 * h][2], acc[q][2 * h][3]);
                v[2] = pack2(acc[q][2 * h + 1][0], acc[q][2 * h + 1][1]); v[3] = pack2(acc[q][2 * h + 1][2], acc[q][2 * h + 1][3]);
                __builtin_amdgcn_raw_buffer_store_b128(v, xr, xo + (q * 4 + ksl) * 8192 + h * 1024, 0, 16);
            }
        }
    }
    f32x4 own[4];
#pragma unroll
    for (int n = 0; n < 4; ++n) own[n] = ksl == 0 ? acc[0][n] : (ksl == 1 ? acc[1][n] : (ksl == 2 ? acc[2][n] : acc[3][n]));
    WAITV(0);
    BAR();
    if (tid == 0) {
        __hip_atomic_fetch_add(cnt + tile * 16, 1u, __ATOMIC_RELAXED, __HIP_MEMORY_SCOPE_AGENT);
        for (unsigned spin = 0; spin < (1u << 24); ++spin) {
            if (__hip_atomic_load(cnt + tile * 16, __ATOMIC_RELAXED, __HIP_MEMORY_SCOPE_AGENT) >= 4u) break;
            __builtin_amdgcn_s_sleep(1);
        }
    }
    BAR();
    {
        u32x4 pv[3][2];
#pragma unroll
        for (int i = 0; i < 3; ++i) {
            const int p = (ksl + 1 + i) & 3;
#pragma unroll
            for (int h = 0; h < 2; ++h) pv[i][h] = __builtin_amdgcn_raw_buffer_load_b128(xr, xo + (ksl * 4 + p) * 8192 + h * 1024, 0, 16);
        }
#pragma unroll
        for (int i = 0; i < 3; ++i)
#pragma unroll
            for (int n = 0; n < 4; ++n)
#pragma unroll
                for (int e = 0; e < 4; ++e) {
                    const unsigned u = pv[i][n >> 1][(n & 1) * 2 + (e >> 1)];
                    own[n][e] += __uint_as_float((e & 1) ? (u & 0xffff0000u) : (u << 16));
                }
    }
#pragma unroll
    for (int n = 0; n < 4; ++n) {
        float r[4];
#pragma unroll
        for (int e = 0; e < 4; ++e)
            r[e] = own[n][e] + bf2f((u16)(xv[n][e >> 1] >> ((e & 1) * 16))) + bf2f((u16)(bv[n][e >> 1] >> ((e & 1) * 16)));
        u32x2 o;
        o[0] = pack2(r[0], r[1]);
        o[1] = pack2(r[2], r[3]);
        *reinterpret_cast<u32x2*>(out + (size_t)row * D_ + col + n * 16) = o;
    }
}

extern "C" void kernel_launch(void* const* d_in, const int* in_sizes, int n_in,
                              void* d_out, int out_size, void* d_ws, size_t ws_size,
                              hipStream_t stream) {
    const u16* x = (const u16*)d_in[0];
    const u16* Cr = (const u16*)d_in[1];
    const u16* Ci = (const u16*)d_in[2];
    const u16* Dr = (const u16*)d_in[3];
    const u16* Di = (const u16*)d_in[4];
    const u16* w1 = (const u16*)d_in[6];
    const u16* b1 = (const u16*)d_in[7];
    const u16* w2 = (const u16*)d_in[8];
    const u16* b2 = (const u16*)d_in[9];
    u16* out = (u16*)d_out;
    char* ws = (char*)d_ws;
    u16* BT = (u16*)ws;
    u16* H = (u16*)(ws + 6291456);
    float* XB = (float*)(ws + 6291456 + 7340032);
    unsigned* cnt = (unsigned*)(ws + 6291456 + 7340032 + 16777216);
    (void)hipFuncSetAttribute((const void*)k1, hipFuncAttributeMaxDynamicSharedMemorySize, K1_LDS);
    (void)hipFuncSetAttribute((const void*)k2, hipFuncAttributeMaxDynamicSharedMemorySize, 131072);
    k1<<<256, 512, K1_LDS, stream>>>(x, Dr, Di, w1, b1, w2, BT, H, cnt, Cr, Ci);
    k2<<<256, 256 + 64 * NLD, 131072, stream>>>(Cr, Ci, H, BT, XB, cnt, x, b2, out);
}
```

```cpp
#include <hip/hip_runtime.h>
#include <hip/hip_bf16.h>
#include <stdint.h>

typedef __attribute__((ext_vector_type(8))) short bf16x8;
typedef __attribute__((ext_vector_type(4))) short bf16x4;
typedef __attribute__((ext_vector_type(4))) float f32x4;
typedef __attribute__((ext_vector_type(2))) __bf16 bfx2;
typedef __attribute__((ext_vector_type(4))) unsigned u32x4;
typedef __attribute__((ext_vector_type(2))) unsigned u32x2;
typedef unsigned short u16;
#define LDSP(p) ((__attribute__((address_space(3))) void*)(p))

#define NLD 4
#define LPRIO 3
#define PS 2
#define PC 2
#define S_ 2048
#define D_ 512
#define F_ 1792
#define KT_ 5888

__device__ __forceinline__ float bf2f(u16 v) { return __uint_as_float(((unsigned)v) << 16); }
__device__ __forceinline__ unsigned pack2(float lo, float hi) {
    bfx2 r = {(__bf16)lo, (__bf16)hi};
    return __builtin_bit_cast(unsigned, r);
}
__device__ __forceinline__ float rbf(float f) { return (float)(__bf16)f; }
__device__ __forceinline__ bf16x8 sel8(int c, bf16x8 a, bf16x8 b) {
    const u32x4 x = __builtin_bit_cast(u32x4, a), y = __builtin_bit_cast(u32x4, b);
    u32x4 r;
    r[0] = c ? x[0] : y[0]; r[1] = c ? x[1] : y[1]; r[2] = c ? x[2] : y[2]; r[3] = c ? x[3] : y[3];
    return __builtin_bit_cast(bf16x8, r);
}
#define ST16(p, v) asm volatile("global_store_dwordx4 %0, %1, off sc0 sc1\n\ts_nop 1" :: "v"(p), "v"(v) : "memory")
#define WAITV(n) asm volatile("s_waitcnt vmcnt(" #n ")" ::: "memory")
#define BAR() do { asm volatile("" ::: "memory"); __builtin_amdgcn_s_barrier(); asm volatile("" ::: "memory"); } while (0)
#define GLDS(src, dst) __builtin_amdgcn_global_load_lds((const void*)(src), LDSP(dst), 16, 0, 0)
#define MFMA(a, b, c) __builtin_amdgcn_mfma_f32_16x16x32_bf16(a, b, c, 0, 0, 0)

#define K1_STG 40960
#define K1_STATS (3 * K1_STG)
#define K1_CS (K1_STATS + 1024)
#define K1_B1 (K1_CS + 1024)
#define K1_W2 (K1_B1 + 1024)
#define K1_PF (K1_W2 + 8192)
#define K1_LDS (K1_PF + 256)

__device__ __forceinline__ void k1_loader(char* smem, const u16* __restrict__ x, const u16* __restrict__ Dr,
                                          const u16* __restrict__ Di, const u16* __restrict__ w1,
                                          bool isd, int rb, int n0, int nu, int L, int lane) {
    __builtin_amdgcn_s_setprio(3);
    const unsigned r0 = lane >> 3, c = lane & 7, hb = (lane >> 4) & 1;
    const bool wt = (L != 0) && !isd;
    unsigned so[4];
#pragma unroll
    for (int q = 0; q < 4; ++q) {
        const unsigned sw = L == 0 ? ((q << 1) | hb) : (isd ? (((q & 1) * 4 + (lane >> 4)) & 7) : ((hb | ((q & 1) << 1)) << 1));
        so[q] = r0 * (wt ? F_ * 2 : D_ * 2) + (c ^ sw) * 16;
    }
    const int rr = n0 + (L - 1) * 96;
    uint64_t pLo, pHi;
    int cross = 64;
    if (L == 0) pLo = pHi = (uint64_t)(x + (size_t)rb * 128 * D_);
    else if (isd) {
        pLo = (uint64_t)Dr + (uint64_t)rr * (D_ * 2);
        pHi = (uint64_t)Di + (uint64_t)rr * (D_ * 2) - 512 * (D_ * 2);
        cross = (512 - rr) >> 5;
    } else pLo = pHi = (uint64_t)(w1 + n0);
    const int ng = L == 0 ? 4 : (L == 1 ? 3 : (nu == 12 ? 3 : 1));
    const int g0 = L == 2 ? 3 : 0;
    const unsigned es = wt ? 8 * F_ * 2 : 8 * D_ * 2;
    const unsigned js = wt ? 64 * F_ * 2 : 128;
    char* dbase = smem + (L == 0 ? 0 : (L == 1 ? 16384 : 16384 + 12288));
    int buf = 0;
#pragma unroll 1
    for (int j = -2; j < 8; ++j) {
        if (j >= 0) {
            if (j == 7) WAITV(0); else if (ng == 4) WAITV(16); else if (ng == 3) WAITV(12); else WAITV(4);
            BAR();
        }
        if (j + 2 < 8) {
            char* dst = dbase + buf * K1_STG;
            buf = buf == 2 ? 0 : buf + 1;
#pragma unroll 1
            for (int g = 0; g < ng; ++g) {
                const int G = g + g0;
                const uint64_t b = (g < cross ? pLo : pHi) + (uint64_t)(j + 2) * js +
                                   (wt ? (unsigned)((G & 1) * 4 * (8 * F_ * 2) + (G >> 1) * 128) : (unsigned)(g * 32 * (D_ * 2)));
#pragma unroll
                for (int e = 0; e < 4; ++e) GLDS((const char*)(b + e * es) + so[e], dst + (g * 4 + e) * 1024);
            }
        }
    }
}

template <bool ISD>
__device__ __forceinline__ void k1_body(char* smem, int nu, int w, int lane) {
    const int fr = lane & 15, fq = lane >> 4;
    const int wm = w >> 1, wn = w & 1;
    const unsigned aoff = (wm * 64 + ((fr >> 2) << 3) + (fr & 3)) * 128 + ((fq ^ ((fr >> 1) & 7)) * 16);
    unsigned boff[2];
#pragma unroll
    for (int e = 0; e < 2; ++e) {
        const int u = wn * 2 + e;
        if (ISD) boff[e] = 16384 + (u * 16 + fr) * 128 + ((fq ^ ((fr >> 1) & 7)) * 16);
        else {
            const int q = fr >> 2, p = fr & 3;
            const int f = ((q >> 1) & 1) | ((fq & 1) << 1);
            boff[e] = 16384 + (fq * 8 + q) * 128 + ((u ^ f) * 32) + p * 8;
        }
    }
    f32x4 acc[4][6] = {};
    const bf16x8 ones = {0x3f80, 0x3f80, 0x3f80, 0x3f80, 0x3f80, 0x3f80, 0x3f80, 0x3f80};
    f32x4 cs[3] = {}, s1[2] = {}, s2[2] = {};
    bf16x8 a0[4], b0[6], a1[4], b1f[6];
#define K1_LDF(af, bfr, j, kk)                                                                                   \
    do {                                                                                                         \
        const char* sb = smem + ((j) % 3) * K1_STG;                                                              \
        _Pragma("unroll") for (int m = 0; m < 4; ++m)                                                            \
            af[m] = *reinterpret_cast<const bf16x8*>(sb + (aoff ^ ((kk) * 64)) + (m >> 1) * 4096 + (m & 1) * 512); \
        _Pragma("unroll") for (int i = 0; i < 6; ++i) {                                                          \
            if (ISD) bfr[i] = *reinterpret_cast<const bf16x8*>(sb + (boff[i & 1] ^ ((kk) * 64)) + (i >> 1) * 8192); \
            else {                                                                                               \
                bf16x4 h0 = __builtin_amdgcn_ds_read_tr16_b64_v4i16((__attribute__((address_space(3))) bf16x4*)(sb + boff[i & 1] + (i >> 1) * 8192 + (kk) * 4096));        \
                bf16x4 h1 = __builtin_amdgcn_ds_read_tr16_b64_v4i16((__attribute__((address_space(3))) bf16x4*)(sb + boff[i & 1] + (i >> 1) * 8192 + (kk) * 4096 + 512));  \
                bfr[i] = __builtin_shufflevector(h0, h1, 0, 1, 2, 3, 4, 5, 6, 7);                                \
            }                                                                                                    \
        }                                                                                                        \
    } while (0)
#define K1_MMA(af, bfr)                                                                                          \
    do {                                                                                                         \
        _Pragma("unroll") for (int i = 0; i < 6; ++i)                                                            \
            _Pragma("unroll") for (int m = 0; m < 4; ++m)                                                        \
                acc[m][i] = ISD ? MFMA(af[m], bfr[i], acc[m][i]) : MFMA(bfr[i], af[m], acc[m][i]);               \
        _Pragma("unroll") for (int t = 0; t < 2; ++t) {                                                          \
            const bf16x8 sa_ = sel8(wn, af[2 + t], af[t]);                                                           \
            s1[t] = MFMA(sa_, ones, s1[t]); s2[t] = MFMA(sa_, sa_, s2[t]);                                       \
        }                                                                                                        \
        _Pragma("unroll") for (int i = 0; i < 3; ++i) {                                                          \
            const bf16x8 sb_ = sel8(wm, bfr[3 + i], bfr[i]);                                                         \
            cs[i] = ISD ? MFMA(ones, sb_, cs[i]) : MFMA(sb_, ones, cs[i]);                                       \
        }                                                                                                        \
        _Pragma("unroll") for (int q = 0; q < 31; ++q) {                                                         \
            __builtin_amdgcn_sched_group_barrier(0x008, 1, 0);                                                   \
            if (q < (ISD ? 10 : 16)) __builtin_amdgcn_sched_group_barrier(0x100, 1, 0);                          \
            if (q < 20) __builtin_amdgcn_sched_group_barrier(0x002, 1, 0);                                       \
        }                                                                                                        \
    } while (0)
    BAR();
    K1_LDF(a0, b0, 0, 0);
#pragma unroll 1
    for (int j = 0; j < 7; ++j) {
        K1_LDF(a1, b1f, j, 1);
        K1_MMA(a0, b0);
        BAR();
        K1_LDF(a0, b0, j + 1, 0);
        K1_MMA(a1, b1f);
    }
    K1_LDF(a1, b1f, 7, 1);
    K1_MMA(a0, b0);
    K1_MMA(a1, b1f);
    BAR();
    float* stats = (float*)(smem + K1_STATS);
    float* csp = (float*)(smem + K1_CS);
    if ((fr >> 2) == fq) {
        const int e = fr & 3;
#pragma unroll
        for (int t = 0; t < 2; ++t) {
            const float a = e == 0 ? s1[t][0] : (e == 1 ? s1[t][1] : (e == 2 ? s1[t][2] : s1[t][3]));
            const float b = e == 0 ? s2[t][0] : (e == 1 ? s2[t][1] : (e == 2 ? s2[t][2] : s2[t][3]));
            const float mean = a * (1.0f / D_);
            const float var = fmaxf(b * (1.0f / D_) - mean * mean, 0.f);
            const float sd = rbf(sqrtf(rbf(rbf(var) + 1e-5f)));
            const int r = wm * 64 + wn * 32 + ((fr >> 2) << 3) + 4 * t + (fr & 3);
            stats[r * 2] = rbf(mean); stats[r * 2 + 1] = 1.0f / sd;
        }
    }
#pragma unroll
    for (int i = 0; i < 3; ++i) {
        const int ii = wm * 3 + i;
        float* d = csp + ((ii >> 1) * 4 + wn * 2 + (ii & 1)) * 16;
        if (ISD) { if (fq == 0) d[fr] = cs[i][0]; }
        else { if (fr == 0) *reinterpret_cast<f32x4*>(d + fq * 4) = cs[i]; }
    }
    {
        char* wb = ISD ? smem + (wn * 32 + fr) * 528 + (wm * 64 + fq * 8) * 4
                       : smem + (wm * 64 + ((fr >> 2) << 3) + (fr & 3)) * 784 + (wn * 32 + fq * 4) * 4;
#pragma unroll
        for (int i = 0; i < 6; ++i)
#pragma unroll
            for (int m = 0; m < 4; ++m) {
                const int uo = (i >> 1) * 4 + (i & 1);
                if (ISD) *reinterpret_cast<f32x4*>(wb + uo * 16 * 528 + (m >> 1) * 128 + (m & 1) * 16) = acc[m][i];
                else *reinterpret_cast<f32x4*>(wb + ((m >> 1) * 32 + (m & 1) * 4) * 784 + uo * 64) = acc[m][i];
            }
    }
    asm volatile("s_waitcnt lgkmcnt(0)" ::: "memory");
}

template <bool ISD>
__device__ __forceinline__ void k1_out(char* smem, u16* __restrict__ BT, u16* __restrict__ H, int rb, int n0, int nu, int w, int lane) {
    const float* stats = (const float*)(smem + K1_STATS);
    const float* csp = (const float*)(smem + K1_CS);
    const int nh = nu >> 1;
    if (ISD) {
        const int c = lane & 15;
        f32x4 st[4];
#pragma unroll
        for (int e = 0; e < 4; ++e) st[e] = *reinterpret_cast<const f32x4*>(stats + c * 16 + e * 4);
#pragma unroll 2
        for (int q = 0; q < nh; ++q) {
            const int e0 = (q * 8 + w) * 4 + (lane >> 4);
            const f32x4 p0 = *reinterpret_cast<const f32x4*>(smem + e0 * 528 + c * 32);
            const f32x4 p1 = *reinterpret_cast<const f32x4*>(smem + e0 * 528 + c * 32 + 16);
            const float cc = csp[e0];
            const int nn = n0 + e0;
            const float sg = nn < 512 ? 1.f : -1.f;
            float v[8];
#pragma unroll
            for (int e = 0; e < 8; ++e) {
                const float mu = st[e >> 1][(e & 1) * 2], ri = st[e >> 1][(e & 1) * 2 + 1];
                v[e] = ((e < 4 ? p0[e & 3] : p1[e & 3]) - mu * cc) * (sg * ri);
            }
            u32x4 o;
#pragma unroll
            for (int e = 0; e < 4; ++e) o[e] = pack2(v[2 * e], v[2 * e + 1]);
            ST16(BT + (size_t)(nn & 511) * KT_ + (nn < 512 ? 0 : 2048) + rb * 128 + c * 8, o);
        }
    } else {
#pragma unroll 2
        for (int q = 0; q < nh; ++q) {
            const int id = q * 8 + (lane >> 3);
            const int ln = nu == 12 ? id % 3 : id & 1;
            const int r = w * 16 + (nu == 12 ? id / 3 : id >> 1);
            const int f0 = ln * 64 + (lane & 7) * 8;
            const f32x4 p0 = *reinterpret_cast<const f32x4*>(smem + r * 784 + f0 * 4);
            const f32x4 p1 = *reinterpret_cast<const f32x4*>(smem + r * 784 + f0 * 4 + 16);
            const f32x4 c0 = *reinterpret_cast<const f32x4*>(csp + f0);
            const f32x4 c1 = *reinterpret_cast<const f32x4*>(csp + f0 + 4);
            const u32x4 bv = *reinterpret_cast<const u32x4*>(smem + K1_B1 + f0 * 2);
            const float mu = stats[r * 2], ri = stats[r * 2 + 1];
            float v[8];
#pragma unroll
            for (int e = 0; e < 8; ++e) {
                const float bb = bf2f((u16)(bv[e >> 1] >> ((e & 1) * 16)));
                float z = ((e < 4 ? p0[e & 3] : p1[e & 3]) - mu * (e < 4 ? c0[e & 3] : c1[e & 3])) * ri + bb;
                z = fmaxf(z, 0.f);
                v[e] = z * z;
            }
            u32x4 o;
#pragma unroll
            for (int e = 0; e < 4; ++e) o[e] = pack2(v[2 * e], v[2 * e + 1]);
            ST16(H + (size_t)(rb * 128 + r) * F_ + n0 + f0, o);
        }
    }
}

__global__ __launch_bounds__(512, 2) void k1(const u16* __restrict__ x, const u16* __restrict__ Dr,
                                             const u16* __restrict__ Di, const u16* __restrict__ w1,
                                             const u16* __restrict__ b1, const u16* __restrict__ w2,
                                             u16* __restrict__ BT, u16* __restrict__ H, unsigned* __restrict__ cnt,
                                             const u16* __restrict__ Cr, const u16* __restrict__ Ci) {
    extern __shared__ __attribute__((aligned(16))) char smem[];
    const int tid = threadIdx.x, lane = tid & 63, w = __builtin_amdgcn_readfirstlane(tid >> 6);
    const int bid = blockIdx.x, xcd = bid & 7, idx = bid >> 3;
    const int rb = (xcd & 1) * 8 + (idx >> 2);
    const int ct = (xcd >> 1) * 4 + (idx & 3);
    if (bid == 255 && w == 7) cnt[lane * 16] = 0u;
    const int fb = bid >> 3, eb = bid & 7;
    const bool light = (ct & 3) == 3;
    const int hv = ct - (ct >> 2);
    const bool isd = light ? ct < 8 : hv < 4;
    const int n0 = 16 * (light ? (isd ? 48 + 8 * (ct >> 2) : 96 + 8 * ((ct >> 2) - 2)) : (isd ? 12 * hv : 12 * (hv - 4)));
    const int nu = light ? 8 : 12;
    if (w >= 4) {
        if (w == 7) {
            if (bid < 224) {
#pragma unroll
                for (int i = 0; i < 8; ++i)
                    GLDS(w2 + (size_t)(fb * 64 + i * 8 + (lane >> 3)) * D_ + eb * 64 + (lane & 7) * 8, smem + K1_W2 + i * 1024);
            }
            if (!isd && lane < nu * 2) GLDS(b1 + n0 + lane * 8, smem + K1_B1);
            {
                const int sl = xcd >> 1;
                const int nl = sl < 2 ? 23 : 18;
                const int row0 = (xcd & 1) * 1024 + idx * 32;
                const bool dopf = xcd < 6;
#pragma unroll 1
                for (int j = 0; j < 8; ++j) {
                    BAR();
                    if (dopf && j >= PS) {
#pragma unroll 1
                        for (int i = 0; i < PC; ++i) {
                            int t = ((j - PS) * PC + i) * 64 + lane;
                            if (t >= 32 * nl) t = lane;
                            const int r = t / nl, l = t - r * nl;
                            const int kb = sl * 23 + l;
                            const u16* p = (kb < 32 ? Cr + kb * 64 : Ci + (kb - 32) * 64) + (size_t)(row0 + r) * S_;
                            __builtin_amdgcn_global_load_lds((const void*)p, LDSP(smem + K1_PF), 4, 0, 0);
                        }
                    }
                }
                if (dopf) WAITV(12); else WAITV(0);
            }
        } else {
            k1_loader(smem, x, Dr, Di, w1, isd, rb, n0, nu, w - 4, lane);
            __builtin_amdgcn_s_setprio(0);
        }
        BAR();
        if (bid < 224) {
            const int fr = lane & 15, fq = lane >> 4, q = fr >> 2, p = fr & 3;
            const int eu = w & 3;
#pragma unroll
            for (int fh = 0; fh < 2; ++fh) {
                bf16x4 h[2];
#pragma unroll
                for (int t2 = 0; t2 < 2; ++t2) {
                    const int f = fh * 32 + fq * 8 + t2 * 4 + q;
                    h[t2] = __builtin_amdgcn_ds_read_tr16_b64_v4i16(
                        (__attribute__((address_space(3))) bf16x4*)(smem + K1_W2 + f * 128 + eu * 32 + p * 8));
                }
                const u32x4 o = __builtin_bit_cast(u32x4, __builtin_shufflevector(h[0], h[1], 0, 1, 2, 3, 4, 5, 6, 7));
                ST16(BT + (size_t)(eb * 64 + eu * 16 + fr) * KT_ + 4096 + fb * 64 + fh * 32 + fq * 8, o);
            }
        }
    } else {
        if (isd) k1_body<true>(smem, nu, w, lane);
        else k1_body<false>(smem, nu, w, lane);
    }
    BAR();
    if (isd) k1_out<true>(smem, BT, H, rb, n0, nu, w, lane);
    else k1_out<false>(smem, BT, H, rb, n0, nu, w, lane);
    WAITV(0);
}

__global__ __launch_bounds__(256 + 64 * NLD, 2) void k2(const u16* __restrict__ Cr, const u16* __restrict__ Ci,
                                             const u16* __restrict__ H, const u16* __restrict__ BT,
                                             float* __restrict__ XB, unsigned* __restrict__ cnt,
                                             const u16* __restrict__ x, const u16* __restrict__ b2,
                                             u16* __restrict__ out) {
    extern __shared__ __attribute__((aligned(16))) char smem[];
    const int tid = threadIdx.x, lane = tid & 63, w = __builtin_amdgcn_readfirstlane(tid >> 6);
    const int bid = blockIdx.x, xcd = bid & 7, idx = bid >> 3;
    const int ksl = xcd >> 1;
    const int rb = (xcd & 1) * 8 + (idx >> 2);
    const int nt = idx & 3;
    constexpr int NK = 23;
    if (w >= 4) {
        const bool isA = ((w - 4) < NLD / 2);
        const int part = (w - 4) % (NLD / 2);
        __builtin_amdgcn_s_setprio(LPRIO);
        const uint64_t pA0 = (uint64_t)(Cr + (size_t)(rb * 128) * S_);
        const uint64_t pA1 = (uint64_t)(Ci + (size_t)(rb * 128) * S_) - 32 * 128;
        const uint64_t pA2 = (uint64_t)(H + (size_t)(rb * 128) * F_) - 64 * 128;
        const uint64_t pB = (uint64_t)(BT + (size_t)(nt * 128) * KT_);
        const unsigned r0 = lane >> 3;
        const unsigned c0 = ((lane & 7) ^ ((lane >> 4) & 7)) * 16, c1 = ((lane & 7) ^ ((4 + (lane >> 4)) & 7)) * 16;
#define K2_LOAD(j)                                                                   \
    do {                                                                             \
        const int kb = ksl * NK + (j);                                               \
        char* dst = smem + ((j) & 3) * 32768 + (isA ? 0 : 16384) + part * (32 / NLD) * 1024; \
        uint64_t base = kb < 32 ? pA0 : (kb < 64 ? pA1 : pA2);                       \
        unsigned ldb = kb < 64 ? S_ * 2 : F_ * 2;                                    \
        if (!isA) { base = pB; ldb = KT_ * 2; }                                      \
        base += kb * 128 + (uint64_t)(part * (32 / NLD) * 8) * ldb;                  \
        const unsigned o0 = r0 * ldb + c0, o1 = r0 * ldb + c1;                       \
        _Pragma("unroll") for (int i = 0; i < 32 / NLD; ++i)                         \
            GLDS((const char*)(base + (uint64_t)(i * 8) * ldb) + ((i & 1) ? o1 : o0), dst + i * 1024); \
    } while (0)
        K2_LOAD(0); K2_LOAD(1); K2_LOAD(2);
        if (NLD == 2) WAITV(32); else WAITV(16);
        BAR();
#pragma unroll 1
        for (int j = 0; j < NK - 1; ++j) {
            if (j + 2 < NK) { if (NLD == 2) WAITV(16); else WAITV(8); } else WAITV(0);
            BAR();
            if (j + 3 < NK) K2_LOAD(j + 3);
        }
        return;
    }
    const int fr = lane & 15, fq = lane >> 4;
    const int wm = w >> 1, wn = w & 1;
    f32x4 acc[4][4] = {};
    const unsigned aoff = (wm * 64 + fr) * 128 + ((fq ^ ((fr >> 1) & 7)) * 16);
    const unsigned boff = 16384 + (wn * 64 + fr) * 128 + ((fq ^ ((fr >> 1) & 7)) * 16);
    bf16x8 a0[4], b0[4], a1[4], b1[4];
#define K2_LDF(af, bf, j, kk)                                                                                  \
    do {                                                                                                       \
        const char* sa = smem + ((j) & 3) * 32768;                                                             \
        af[0] = *reinterpret_cast<const bf16x8*>(sa + (aoff ^ ((kk) * 64)));                                   \
        _Pragma("unroll") for (int m = 0; m < 4; ++m)                                                          \
            bf[m] = *reinterpret_cast<const bf16x8*>(sa + (boff ^ ((kk) * 64)) + m * 2048);                    \
        _Pragma("unroll") for (int m = 1; m < 4; ++m)                                                          \
            af[m] = *reinterpret_cast<const bf16x8*>(sa + (aoff ^ ((kk) * 64)) + m * 2048);                    \
    } while (0)
#define K2_MMA(af, bf)                                                               \
    do {                                                                             \
        _Pragma("unroll") for (int m = 0; m < 4; ++m)                                \
            _Pragma("unroll") for (int n = 0; n < 4; ++n) acc[m][n] = MFMA(bf[n], af[m], acc[m][n]); \
    } while (0)
#define K2_SCHED()                                                                   \
    do {                                                                             \
        _Pragma("unroll") for (int q = 0; q < 8; ++q) {                              \
            __builtin_amdgcn_sched_group_barrier(0x100, 1, 0);                       \
            __builtin_amdgcn_sched_group_barrier(0x008, 2, 0);                       \
        }                                                                            \
    } while (0)
    const int row = rb * 128 + wm * 64 + ksl * 16 + fr;
    const int col = nt * 128 + wn * 64 + fq * 4;
    u32x2 xv[4], bv[4];
#pragma unroll
    for (int n = 0; n < 4; ++n) {
        xv[n] = *reinterpret_cast<const u32x2*>(x + (size_t)row * D_ + col + n * 16);
        bv[n] = *reinterpret_cast<const u32x2*>(b2 + col + n * 16);
    }
    BAR();
    K2_LDF(a0, b0, 0, 0);
#pragma unroll 1
    for (int j = 0; j < NK - 1; ++j) {
        K2_LDF(a1, b1, j, 1);
        K2_MMA(a0, b0);
        K2_SCHED();
        BAR();
        K2_LDF(a0, b0, j + 1, 0);
        K2_MMA(a1, b1);
        K2_SCHED();
    }
    K2_LDF(a1, b1, NK - 1, 1);
    K2_MMA(a0, b0);
    K2_MMA(a1, b1);
    const int tile = rb * 4 + nt;
    const __amdgpu_buffer_rsrc_t xr = __builtin_amdgcn_make_buffer_rsrc((void*)XB, 0, 64 * 131072, 0x00020000);
    const unsigned xo = (unsigned)tile * 131072u + (unsigned)w * 2048u + (unsigned)lane * 16u;
#pragma unroll
    for (int q = 0; q < 4; ++q) {
        if (q != ksl) {
#pragma unroll
            for (int h = 0; h < 2; ++h) {
                u32x4 v;
                v[0] = pack2(acc[q][2 * h][0], acc[q][2 * h][1]); v[1] = pack2(acc[q][2 * h][2], acc[q][2 * h][3]);
                v[2] = pack2(acc[q][2 * h + 1][0], acc[q][2 * h + 1][1]); v[3] = pack2(acc[q][2 * h + 1][2], acc[q][2 * h + 1][3]);
                __builtin_amdgcn_raw_buffer_store_b128(v, xr, xo + (q * 4 + ksl) * 8192 + h * 1024, 0, 16);
            }
        }
    }
    f32x4 own[4];
#pragma unroll
    for (int n = 0; n < 4; ++n) own[n] = ksl == 0 ? acc[0][n] : (ksl == 1 ? acc[1][n] : (ksl == 2 ? acc[2][n] : acc[3][n]));
    WAITV(0);
    BAR();
    if (tid == 0) {
        __hip_atomic_fetch_add(cnt + tile * 16, 1u, __ATOMIC_RELAXED, __HIP_MEMORY_SCOPE_AGENT);
        for (unsigned spin = 0; spin < (1u << 24); ++spin) {
            if (__hip_atomic_load(cnt + tile * 16, __ATOMIC_RELAXED, __HIP_MEMORY_SCOPE_AGENT) >= 4u) break;
            __builtin_amdgcn_s_sleep(1);
        }
    }
    BAR();
    {
        u32x4 pv[3][2];
#pragma unroll
        for (int i = 0; i < 3; ++i) {
            const int p = (ksl + 1 + i) & 3;
#pragma unroll
            for (int h = 0; h < 2; ++h) pv[i][h] = __builtin_amdgcn_raw_buffer_load_b128(xr, xo + (ksl * 4 + p) * 8192 + h * 1024, 0, 16);
        }
#pragma unroll
        for (int i = 0; i < 3; ++i)
#pragma unroll
            for (int n = 0; n < 4; ++n)
#pragma unroll
                for (int e = 0; e < 4; ++e) {
                    const unsigned u = pv[i][n >> 1][(n & 1) * 2 + (e >> 1)];
                    own[n][e] += __uint_as_float((e & 1) ? (u & 0xffff0000u) : (u << 16));
                }
    }
#pragma unroll
    for (int n = 0; n < 4; ++n) {
        float r[4];
#pragma unroll
        for (int e = 0; e < 4; ++e)
            r[e] = own[n][e] + bf2f((u16)(xv[n][e >> 1] >> ((e & 1) * 16))) + bf2f((u16)(bv[n][e >> 1] >> ((e & 1) * 16)));
        u32x2 o;
        o[0] = pack2(r[0], r[1]);
        o[1] = pack2(r[2], r[3]);
        *reinterpret_cast<u32x2*>(out + (size_t)row * D_ + col + n * 16) = o;
    }
}

extern "C" void kernel_launch(void* const* d_in, const int* in_sizes, int n_in,
                              void* d_out, int out_size, void* d_ws, size_t ws_size,
                              hipStream_t stream) {
    const u16* x = (const u16*)d_in[0];
    const u16* Cr = (const u16*)d_in[1];
    const u16* Ci = (const u16*)d_in[2];
    const u16* Dr = (const u16*)d_in[3];
    const u16* Di = (const u16*)d_in[4];
    const u16* w1 = (const u16*)d_in[6];
    const u16* b1 = (const u16*)d_in[7];
    const u16* w2 = (const u16*)d_in[8];
    const u16* b2 = (const u16*)d_in[9];
    u16* out = (u16*)d_out;
    char* ws = (char*)d_ws;
    u16* BT = (u16*)ws;
    u16* H = (u16*)(ws + 6291456);
    float* XB = (float*)(ws + 6291456 + 7340032);
    unsigned* cnt = (unsigned*)(ws + 6291456 + 7340032 + 16777216);
    (void)hipFuncSetAttribute((const void*)k1, hipFuncAttributeMaxDynamicSharedMemorySize, K1_LDS);
    (void)hipFuncSetAttribute((const void*)k2, hipFuncAttributeMaxDynamicSharedMemorySize, 131072);
    k1<<<256, 512, K1_LDS, stream>>>(x, Dr, Di, w1, b1, w2, BT, H, cnt, Cr, Ci);
    k2<<<256, 256 + 64 * NLD, 131072, stream>>>(Cr, Ci, H, BT, XB, cnt, x, b2, out);
}
```
